```python
import torch

def _softmax(x, axis=-1):
    return torch.softmax(x, dim=axis)
def _gelu(x):
    return torch.nn.functional.gelu(x, approximate="tanh")
def _silu(x):
    return torch.nn.functional.silu(x)
def _sigmoid(x):
    return torch.sigmoid(x)
def _layernorm(x, axis=-1, eps=1e-5):
    m = x.mean(dim=axis, keepdim=True)
    v = x.var(dim=axis, keepdim=True, unbiased=False)
    return (x - m) / torch.sqrt(v + eps)
def _scatter_add(tgt, idx, src, axis=0):
    return tgt.clone().index_add_(axis, idx, src)
def _segment_sum(src, seg, n):
    out = torch.zeros((n,) + src.shape[1:], dtype=src.dtype)
    return out.index_add_(0, seg, src)
def _pad(x, pw):
    flat = tuple(v for pair in reversed(pw) for v in pair)
    return torch.nn.functional.pad(x, flat)

kernel_name = 'recurrent_scan__sliding_local_dilated__swiglu_ffn__prompt'

def setup_inputs():

    _w = (lambda a: (lambda t: t.to(torch.bfloat16) if t.is_floating_point() else t)(torch.as_tensor(a)))
    def _A_setup():
        B, S, D = 1, 2048, 576
        return {
            "x": _w((lambda *s: torch.randn(*s, dtype=torch.bfloat16) * 0.05)(B, S, D)),
            "wqkv": _w((lambda *s: torch.randn(*s, dtype=torch.bfloat16) * 0.05)(D, 3 * D)),
            "wo": _w((lambda *s: torch.randn(*s, dtype=torch.bfloat16) * 0.05)(D, D)),
        }
    _A_in = _A_setup()

    def _B_setup():
        B, S, D, F = 1, 2048, 576, 4608
        return {
            "x": _w((lambda *s: torch.randn(*s, dtype=torch.bfloat16) * 0.05)(B, S, D)),
            "w_gate": _w((lambda *s: torch.randn(*s, dtype=torch.bfloat16) * 0.05)(D, F)),
            "w_up": _w((lambda *s: torch.randn(*s, dtype=torch.bfloat16) * 0.05)(D, F)),
            "w_down": _w((lambda *s: torch.randn(*s, dtype=torch.bfloat16) * 0.05)(F, D)),
        }
    _B_in = _B_setup()

    return {
        "A_x": _A_in["x"],
        "A_wqkv": _A_in["wqkv"],
        "A_wo": _A_in["wo"],
        "B_x": _B_in["x"],
        "B_w_gate": _B_in["w_gate"],
        "B_w_up": _B_in["w_up"],
        "B_w_down": _B_in["w_down"],
    }

def reference(A_x, A_wqkv, A_wo, B_x, B_w_gate, B_w_up, B_w_down):
    def _A_ref(x, wqkv, wo):
        B, S, D = x.shape
        H = 32; Dh = D // H
        W, dil = 8, 2
        qkv = (lambda x, *d: x.permute(*d))((x @ wqkv).reshape(B, S, 3, H, Dh), 2, 0, 3, 1, 4)
        q, k, v = qkv[0], qkv[1], qkv[2]
        scale = Dh ** -0.5
        pos = torch.arange(S)
        off = torch.arange(W) * dil
        idx = pos[:, None] - off[None, :]
        mask = idx >= 0
        idx_c = torch.where(mask, idx, 0).reshape(-1)
        kg = k[:, :, idx_c, :].reshape(B, H, S, W, Dh)
        vg = v[:, :, idx_c, :].reshape(B, H, S, W, Dh)
        logits = torch.einsum("bhsd,bhswd->bhsw", q, kg) * scale
        logits = torch.where(mask[None, None, :, :], logits, -1e9)
        attn = _softmax(logits, axis=-1)
        o = torch.einsum("bhsw,bhswd->bhsd", attn, vg)
        return (lambda x, *d: x.permute(*d))(o, 0, 2, 1, 3).reshape(B, S, D) @ wo

    def _B_ref(x, w_gate, w_up, w_down):
        g = _silu(x @ w_gate)
        u = x @ w_up
        return (g * u) @ w_down

    x = A_x
    xn = _layernorm(x, axis=-1)
    s = _A_ref(xn, A_wqkv, A_wo)
    g = _silu(_B_ref(xn, B_w_gate, B_w_up, B_w_down))
    return x + s * g
```

```cpp
#include <hip/hip_runtime.h>
#include <hip/hip_bf16.h>
namespace naive {
typedef unsigned short u16;
__device__ __forceinline__ float b2f(u16 v) { return __uint_as_float(((unsigned)v) << 16); }
__device__ __forceinline__ u16 f2b(float f) { unsigned u = __float_as_uint(f); u += 0x7fffu + ((u >> 16) & 1u); return (u16)(u >> 16); }
__device__ __forceinline__ float rb(float f) { return b2f(f2b(f)); }
constexpr int S = 2048, D = 576, F = 4608;
__global__ void ln_k(const u16* x, u16* xn) {
  int row = blockIdx.x, t = threadIdx.x;
  float s = 0; for (int i = t; i < D; i += 64) s += b2f(x[row*D+i]);
  for (int o = 32; o; o >>= 1) s += __shfl_xor(s, o);
  float mean = s / D; float v = 0;
  for (int i = t; i < D; i += 64) { float d = b2f(x[row*D+i]) - mean; v += d*d; }
  for (int o = 32; o; o >>= 1) v += __shfl_xor(v, o);
  float mb = rb(mean), vb = rb(v / D);
  float sd = rb(sqrtf(rb(vb + 1e-5f)));
  for (int i = t; i < D; i += 64) xn[row*D+i] = f2b(rb(b2f(x[row*D+i]) - mb) / sd);
}
__global__ void gemm_k(const u16* A, const u16* B, u16* C, int M, int N, int K) {
  __shared__ float As[16][17], Bs[16][17];
  int tx = threadIdx.x, ty = threadIdx.y, r = blockIdx.y*16+ty, c = blockIdx.x*16+tx;
  float acc = 0;
  for (int k0 = 0; k0 < K; k0 += 16) {
    As[ty][tx] = b2f(A[(size_t)r*K + k0+tx]); Bs[ty][tx] = b2f(B[(size_t)(k0+ty)*N + c]);
    __syncthreads();
    for (int k = 0; k < 16; ++k) acc += As[ty][k]*Bs[k][tx];
    __syncthreads();
  }
  C[(size_t)r*N+c] = f2b(acc);
}
__global__ void attn_k(const u16* qkv, u16* o) {
  int idx = blockIdx.x*blockDim.x+threadIdx.x; int s = idx / 32, h = idx % 32;
  if (s >= S) return;
  float scale = rb(0.23570226039551584f);
  scale = 0.23570226039551584f;
  float lg[8], mx = -3e38f;
  for (int w = 0; w < 8; ++w) { int j = s - 2*w; float a = 0;
    if (j >= 0) { for (int d = 0; d < 18; ++d) a += b2f(qkv[(size_t)s*1728 + h*18+d]) * b2f(qkv[(size_t)j*1728 + 576 + h*18+d]); a = rb(rb(a)*scale); }
    else a = rb(-1e9f);
    lg[w] = a; mx = fmaxf(mx, a); }
  float sum = 0; for (int w = 0; w < 8; ++w) { lg[w] = expf(lg[w]-mx); sum += lg[w]; }
  for (int w = 0; w < 8; ++w) lg[w] = rb(lg[w]/sum);
  for (int d = 0; d < 18; ++d) { float a = 0;
    for (int w = 0; w < 8; ++w) { int j = s - 2*w; if (j < 0) j = 0; a += lg[w]*b2f(qkv[(size_t)j*1728 + 1152 + h*18+d]); }
    o[(size_t)s*576 + h*18+d] = f2b(a); }
}
__global__ void glu_k(const u16* g, const u16* u, u16* h, int n) {
  int i = blockIdx.x*blockDim.x+threadIdx.x; if (i >= n) return;
  float gv = b2f(g[i]); float sg = rb(gv / (1.f + expf(-gv))); h[i] = f2b(sg * b2f(u[i]));
}
__global__ void fin_k(const u16* x, const u16* s, const u16* y, u16* out, int n) {
  int i = blockIdx.x*blockDim.x+threadIdx.x; if (i >= n) return;
  float yv = b2f(y[i]); float g = rb(yv / (1.f + expf(-yv))); out[i] = f2b(b2f(x[i]) + rb(b2f(s[i]) * g));
}
struct Bufs { u16 *xn, *qkv, *o, *s, *g, *u, *h, *y; };
inline Bufs carve(void* ws) { u16* p = (u16*)ws; Bufs b; b.xn = p; p += S*D; b.qkv = p; p += S*1728; b.o = p; p += S*D; b.s = p; p += S*D; b.g = p; p += (size_t)S*F; b.u = p; p += (size_t)S*F; b.h = p; p += (size_t)S*F; b.y = p; return b; }
inline void run(void* const* d_in, void* d_out, void* ws, hipStream_t st) {
  const u16* x = (const u16*)d_in[0]; const u16* wqkv = (const u16*)d_in[1]; const u16* wo = (const u16*)d_in[2];
  const u16* wg = (const u16*)d_in[4]; const u16* wu = (const u16*)d_in[5]; const u16* wd = (const u16*)d_in[6];
  Bufs b = carve(ws);
  ln_k<<<S, 64, 0, st>>>(x, b.xn);
  gemm_k<<<dim3(1728/16, S/16), dim3(16,16), 0, st>>>(b.xn, wqkv, b.qkv, S, 1728, D);
  attn_k<<<S*32/256, 256, 0, st>>>(b.qkv, b.o);
  gemm_k<<<dim3(D/16, S/16), dim3(16,16), 0, st>>>(b.o, wo, b.s, S, D, D);
  gemm_k<<<dim3(F/16, S/16), dim3(16,16), 0, st>>>(b.xn, wg, b.g, S, F, D);
  gemm_k<<<dim3(F/16, S/16), dim3(16,16), 0, st>>>(b.xn, wu, b.u, S, F, D);
  glu_k<<<S*F/256, 256, 0, st>>>(b.g, b.u, b.h, S*F);
  gemm_k<<<dim3(D/16, S/16), dim3(16,16), 0, st>>>(b.h, wd, b.y, S, D, F);
  fin_k<<<S*D/256, 256, 0, st>>>(x, b.s, b.y, (u16*)d_out, S*D);
}
}
extern "C" void kernel_launch(void* const* d_in, const int* in_sizes, int n_in, void* d_out, int out_size, void* d_ws, size_t ws_size, hipStream_t stream) {
  naive::run(d_in, d_out, d_ws, stream);
}
```

```cpp
#include <hip/hip_runtime.h>
#include <hip/hip_bf16.h>
#include <stdint.h>

typedef unsigned short u16;
typedef __attribute__((ext_vector_type(8))) __bf16 bf16x8;
typedef __attribute__((ext_vector_type(2))) __bf16 bf16x2;
typedef __attribute__((ext_vector_type(4))) short s16x4;
typedef __attribute__((ext_vector_type(4))) float f32x4;
typedef __attribute__((ext_vector_type(2))) float f32x2;
typedef __attribute__((ext_vector_type(4))) unsigned u32x4;
typedef __attribute__((ext_vector_type(2))) unsigned u32x2;
#define LAS __attribute__((address_space(3)))
#define GAS __attribute__((address_space(1)))
#define DI __device__ __forceinline__
#define UNROLL _Pragma("unroll")
#define SCHED() __builtin_amdgcn_sched_barrier(0)

template <int I> struct IC { static constexpr int v = I; };
template <int B, int E, class Fn> DI void sfor(Fn&& f) { if constexpr (B < E) { f(IC<B>{}); sfor<B + 1, E>(f); } }

constexpr int S_ = 2048, D_ = 576, F_ = 4608;
constexpr size_t WS_XF = 0;
constexpr size_t WS_HF = WS_XF + (size_t)128 * 18 * 1024;
constexpr size_t WS_OF = WS_HF + (size_t)64 * 144 * 2048;
constexpr size_t WS_WD = WS_OF + (size_t)128 * 18 * 1024;
constexpr size_t WS_WO = WS_WD + (size_t)4 * 144 * 9 * 1024;
constexpr size_t WS_PART = WS_WO + (size_t)36 * 18 * 1024;
constexpr size_t WS_FLAG = WS_PART + (size_t)64 * 16 * 18 * 1024;

DI float bf_lo(unsigned u) { return __uint_as_float(u << 16); }
DI float bf_hi(unsigned u) { return __uint_as_float(u & 0xffff0000u); }
DI unsigned pk_bf16(float a, float b) { f32x2 v = {a, b}; bf16x2 r = __builtin_convertvector(v, bf16x2); return __builtin_bit_cast(unsigned, r); }
DI float rbf(float a) { return bf_lo(pk_bf16(a, a)); }
DI float dot2(unsigned a, unsigned b, float c) { return __builtin_amdgcn_fdot2_f32_bf16(__builtin_bit_cast(bf16x2, a), __builtin_bit_cast(bf16x2, b), c, false); }
DI f32x4 mfma(bf16x8 a, bf16x8 b, f32x4 c) { return __builtin_amdgcn_mfma_f32_16x16x32_bf16(a, b, c, 0, 0, 0); }
template <bool FIRST> DI void mma(f32x4& c, const bf16x8& a, const bf16x8& b) {
  if constexpr (FIRST) asm volatile("v_mfma_f32_16x16x32_bf16 %0, %1, %2, 0" : "=&v"(c) : "v"(a), "v"(b));
  else asm volatile("v_mfma_f32_16x16x32_bf16 %0, %1, %2, %0" : "+v"(c) : "v"(a), "v"(b));
}
DI void mma_drain() { SCHED(); asm volatile("s_nop 7\n\ts_nop 1"); SCHED(); }
DI s16x4 trd(unsigned addr) { return __builtin_amdgcn_ds_read_tr16_b64_v4i16((LAS s16x4*)(size_t)addr); }
DI bf16x8 cat(s16x4 lo, s16x4 hi) { return __builtin_bit_cast(bf16x8, __builtin_shufflevector(lo, hi, 0, 1, 2, 3, 4, 5, 6, 7)); }
DI void glds16(const char* base, unsigned voff, unsigned lds) {
  asm volatile("s_mov_b32 m0, %2\n\ts_nop 0\n\tglobal_load_lds_dwordx4 %0, %1" ::"v"(voff), "s"(base), "s"(lds) : "memory");
}
DI void glds4(const char* base, unsigned voff, unsigned lds) {
  asm volatile("s_mov_b32 m0, %2\n\ts_nop 0\n\tglobal_load_lds_dword %0, %1" ::"v"(voff), "s"(base), "s"(lds) : "memory");
}
DI void glds16_half(const char* base, unsigned voff, unsigned lds) {
  asm volatile("s_mov_b32 m0, %2\n\ts_mov_b32 exec_hi, 0\n\tglobal_load_lds_dwordx4 %0, %1\n\ts_mov_b32 exec_hi, -1" ::"v"(voff), "s"(base), "s"(lds) : "memory");
}
DI void opaque(unsigned& v) { asm volatile("" : "+v"(v)); }
template <int N> DI void wait_vm() { asm volatile("s_waitcnt vmcnt(%0)" ::"n"(N > 63 ? 63 : N) : "memory"); }
DI void barrier() { SCHED(); asm volatile("" ::: "memory"); __builtin_amdgcn_s_barrier(); asm volatile("" ::: "memory"); SCHED(); }
DI void gload16(bf16x8& dst, const char* base, unsigned voff) {
  asm volatile("global_load_dwordx4 %0, %1, %2" : "=v"(dst) : "v"(voff), "s"(base) : "memory");
}
DI void pin(bf16x8& v) { asm volatile("" : "+v"(v)); }
#define STAMP_DECL
#define STAMP(i)
#define STAMP_RT(i)
#define STAMP_OUT(k)
DI float silu_mul(float g, float u) {
  float e = __builtin_amdgcn_exp2f(g * -1.4426950408889634f);
  return g * u * __builtin_amdgcn_rcpf(1.0f + e);
}

DI u32x2 silu_mul4(const f32x4 g, const f32x4 u) {
  f32x4 e = g * -1.4426950408889634f;
  UNROLL for (int i = 0; i < 4; ++i) e[i] = __builtin_amdgcn_exp2f(e[i]);
  e += 1.0f;
  UNROLL for (int i = 0; i < 4; ++i) e[i] = __builtin_amdgcn_rcpf(e[i]);
  const f32x4 h = g * u * e;
  return (u32x2){pk_bf16(h[0], h[1]), pk_bf16(h[2], h[3])};
}

__global__ __launch_bounds__(512) void p0_kernel(const u16* __restrict__ x, const u16* __restrict__ wo, const u16* __restrict__ wd, char* __restrict__ ws) {
  __shared__ __attribute__((aligned(16))) char shm[8 * 4096];
  const int tid = threadIdx.x, lane = tid & 63, w = __builtin_amdgcn_readfirstlane(tid >> 6), b = blockIdx.x;
  if (tid == 0) *(unsigned*)(ws + WS_FLAG + (size_t)b * 128) = 0u;
  if (w == 0) {
    const int row = b * 8 + (lane >> 3), i = lane & 7;
    u32x4 v[9];
    UNROLL for (int j = 0; j < 9; ++j) v[j] = *(const u32x4*)((const char*)x + (size_t)row * 1152 + j * 128 + i * 16);
    float s = 0.f;
    UNROLL for (int j = 0; j < 9; ++j) UNROLL for (int e = 0; e < 4; ++e) s += bf_lo(v[j][e]) + bf_hi(v[j][e]);
    s += __shfl_xor(s, 1); s += __shfl_xor(s, 2); s += __shfl_xor(s, 4);
    const float mean = s * (1.0f / 576.0f);
    float q = 0.f;
    UNROLL for (int j = 0; j < 9; ++j) UNROLL for (int e = 0; e < 4; ++e) { float a = bf_lo(v[j][e]) - mean, c = bf_hi(v[j][e]) - mean; q += a * a + c * c; }
    q += __shfl_xor(q, 1); q += __shfl_xor(q, 2); q += __shfl_xor(q, 4);
    const float mb = rbf(mean), vb = rbf(q * (1.0f / 576.0f));
    const float sd = rbf(sqrtf(rbf(vb + 1e-5f)));
    const float rs = 1.0f / sd;
    UNROLL for (int j = 0; j < 9; ++j) {
      u32x4 o;
      UNROLL for (int e = 0; e < 4; ++e) { unsigned d = pk_bf16(bf_lo(v[j][e]) - mb, bf_hi(v[j][e]) - mb); o[e] = pk_bf16(bf_lo(d) * rs, bf_hi(d) * rs); }
      const int ch = j * 8 + i, ks = ch >> 2, g = ch & 3;
      *(u32x4*)(ws + WS_XF + ((size_t)(row >> 4) * 18 + ks) * 1024 + ((row & 15) + 16 * g) * 16) = o;
    }
    return;
  }
  const int unit = b * 7 + (w - 1);
  if (unit >= 1458) return;
  const bool isd = unit < 1296;
  const int u = isd ? unit : unit - 1296, kap = u / 9, lc = u % 9;
  const unsigned sb = (unsigned)(size_t)(LAS char*)shm + w * 4096;
  UNROLL for (int j = 0; j < 4; ++j) {
    const int rho = j * 8 + (lane >> 3), g = rho >> 3, e = rho & 7, pc = lane & 7;
    int srow;
    if (isd) srow = 32 * kap + 16 * (e >> 2) + 4 * g + (e & 3);
    else if (kap < 16) { const int hd = 2 * kap + (g >> 1), pos = 8 * (g & 1) + e;
      srow = 18 * hd + ((hd & 1) ? (pos < 2 ? 16 + pos : pos - 2) : pos); }
    else { const int hd = 16 * (kap - 16) + 4 * g + (e >> 1); srow = 18 * hd + ((hd & 1) ? 14 : 16) + (e & 1); }
    const int f = ((rho >> 1) & 1) | (((rho >> 3) & 1) << 1);
    const unsigned voff = srow * 1152 + lc * 128 + (((pc >> 1) ^ f) * 32) + (pc & 1) * 16;
    glds16((const char*)(isd ? wd : wo), voff, sb + j * 1024);
  }
  wait_vm<0>();
  const int g = lane >> 4, q = (lane & 15) >> 2, p = lane & 3, f = (q >> 1) | ((g & 1) << 1);
  UNROLL for (int t = 0; t < 4; ++t) {
    const unsigned a = sb + (8 * g + q) * 128 + ((t ^ f) * 32) + 8 * p;
    bf16x8 fr = cat(trd(a), trd(a + 512));
    const int NT = 4 * lc + t, ct = NT / 9, nt = NT % 9;
    size_t dst;
    if (isd) { const int ks = kap < 128 ? kap >> 5 : (kap - 128) >> 2, step = kap < 128 ? kap & 31 : 32 + ((kap - 128) & 3);
      dst = WS_WD + ((((size_t)ct * 4 + ks) * 36 + step) * 9 + nt) * 1024; }
    else dst = WS_WO + (((size_t)ct * 9 + nt) * 18 + kap) * 1024;
    *(bf16x8*)(ws + dst + lane * 16) = fr;
  }
}

namespace k1 {
constexpr int RING = 7, SLOT = 16384, LDS_BYTES = 163840, DX = 3;
constexpr int NST = 27, NIT = 27;
constexpr int ROWB = 144;
constexpr int DUMP = 6 * SLOT, HALO = LDS_BYTES - 18432;
static_assert(DUMP + 272 * ROWB + 16 <= HALO, "aux");
constexpr int HALO_IT = 10;
constexpr int item(int n) { return n < 9 ? n : n < 18 ? n + 1 : n < 24 ? n + 2 : NIT; }
constexpr int stage_at(int i) { return i < 0 ? -1 : i < 9 ? i : i == 9 ? -1 : i < 19 ? i - 1 : i == 19 ? -1 : i < 26 ? i - 2 : -1; }
constexpr int BAR[NIT + 1] =      {1, 1, 1, 1, 1, 1, 1, 1, 0, 0, 0, 0, 1, 1, 1, 1, 1, 1, 0, 0, 0, 0, 1, 0, 1, 0, 0, 1};
constexpr int ISSUE[NST] =        {-4, -3, -2, -1, 0, 1, 2, 3, 4, 4, 5, 6, 7, 8, 12, 12, 13, 13, 14, 14, 15, 16, 17, 22, 22, 23, 23};
constexpr bool late(int n) { return n == 0 || n == 11 || n == 20 || n >= 24; }
constexpr int free_item(int n) { if (n < RING) return -100; int i = item(n - RING) + 1; while (!BAR[i]) ++i; return i; }
constexpr int need_bar(int n) { int b = late(n) ? item(n) : item(n) - 1; while (!BAR[b]) --b; return b; }
constexpr int issue_it(int n) { return ISSUE[n]; }
constexpr int stores_at(int i) { return i == 9 || i == 19 ? 4 : 0; }
constexpr int ndma_at(int i) { int c = 0; for (int n = 0; n < NST; ++n) if (issue_it(n) == i) c += 2; return c; }
constexpr int nx_at(int i) { return (i + DX >= 0 && i + DX < 9) ? 4 : 0; }
constexpr int nhalo_at(int i) { return i == HALO_IT ? 3 : 0; }
constexpr int nld_at(int i) { return ndma_at(i) + nx_at(i) + nhalo_at(i); }
constexpr int FIRST_IT = -4;
constexpr int cum_end(int i) { int c = 0; for (int q = FIRST_IT; q <= i; ++q) c += nld_at(q) + stores_at(q); return c; }
constexpr int op_of_stage(int n) { int p = issue_it(n), c = cum_end(p - 1); for (int m = 0; m <= n; ++m) if (issue_it(m) == p) c += 2; return c; }
constexpr int op_of_x(int j) { int p = j - DX; return cum_end(p - 1) + ndma_at(p) + 4; }
constexpr int op_of_halo() { return cum_end(HALO_IT - 1) + nld_at(HALO_IT); }
constexpr int wait_at(int i) {
  int need = 0;
  for (int n = 0; n < NST; ++n) if (need_bar(n) - 1 == i) { int o = op_of_stage(n); if (o > need) need = o; }
  if (i + 1 >= 0 && i + 1 < 9) { int o = op_of_x(i + 1); if (o > need) need = o; }
  if (i == need_bar(18) - 1) { int o = op_of_halo(); if (o > need) need = o; }
  return need == 0 ? 63 : cum_end(i) - need;
}
constexpr bool sched_ok() {
  for (int n = 0; n < NST; ++n) { if (issue_it(n) > need_bar(n) - 1 || issue_it(n) < FIRST_IT || issue_it(n) < free_item(n)) return false; if (late(n) && !BAR[item(n)]) return false; }
  return HALO_IT <= need_bar(18) - 1 && 20 % RING == 6 && free_item(27) <= 26; }
static_assert(sched_ok(), "schedule");
constexpr unsigned region(int n) { return (n % RING) * SLOT >= 65536 ? 65536u : 0u; }
constexpr int nfrag(int n) { return n < 18 ? 16 : 12; }
constexpr int stage_of_op(int i, int k) { int c = 0; for (int n = 0; n < NST; ++n) if (issue_it(n) == i) { if (k < c + 2) return n; c += 2; } return -1; }
}

__global__ __launch_bounds__(512, 2) void k1_kernel(const u16* __restrict__ wqkv, const u16* __restrict__ wg, const u16* __restrict__ wu, char* __restrict__ ws) {
  using namespace k1;
  extern __shared__ __attribute__((aligned(16))) char shm[];
  const unsigned sb = (unsigned)(size_t)(LAS char*)shm;
  const int tid = threadIdx.x, lane = tid & 63, w = __builtin_amdgcn_readfirstlane(tid >> 6), b = blockIdx.x;
  STAMP_DECL; STAMP_RT(62); STAMP(0);
  const int xcd = b & 7, bi = b >> 3, r = (xcd >> 2) * 4 + (bi & 3), c = (xcd & 3) * 8 + (bi >> 2);
  const int g = lane >> 4, q = (lane & 15) >> 2, p = lane & 3;

  unsigned voff_big, voff_q0, voff_q1, voff_gq;
  { const int row = 8 * w + (lane >> 3), pc = lane & 7, f = ((row >> 1) & 1) | (((row >> 3) & 1) << 1);
    voff_big = row * 9216 + (((pc >> 1) ^ f) * 32) + (pc & 1) * 16; }
  const int jq = w < 7 ? 2 * w : 12;
  auto qoff = [&](int j) { int n = 64 * j + lane, pos = n / 9, ch = n % 9; if (pos > 95) pos = 95;
    const int row = (pos & ~15) + 8 * ((pos >> 3) & 1) + 4 * (pos & 1) + ((pos >> 1) & 3);
    return (unsigned)(row * 3456 + (ch / 3) * 1152 + (ch % 3) * 16); };
  voff_q0 = qoff(jq); voff_q1 = qoff(jq + 1);
  const int jg = w < 6 ? w : 5;
  { const int n = 64 * jg + lane, pos = n >> 1, row = (pos & ~12) | ((pos & 4) << 1) | ((pos & 8) >> 1);
    voff_gq = row * 9216 + (n & 1) * 16; }
  const char* gbase = (const char*)wg; const char* ubase = (const char*)wu;
  const char* qbase = (const char*)wqkv + ((36 * c) & ~15);
  bf16x8 xf[18][2];
  const char* xbase = ws + WS_XF + (size_t)(r * 16 + 2 * w) * 18 * 1024;
  const unsigned l16 = lane * 16;
  auto ld_op = [&](auto ii, auto kk) {
    constexpr int I = decltype(ii)::v, K = decltype(kk)::v;
    if constexpr (K < ndma_at(I)) {
      constexpr int n = stage_of_op(I, K), h = K & 1; constexpr unsigned so = (n % RING) * SLOT;
      if constexpr (n < 18) {
        constexpr int st = n % 9, gi = n / 9;
        glds16((h ? ubase : gbase) + ((size_t)st * 64 * 9216 + (2 * c + gi) * 128), voff_big, sb + so + h * 8192 + w * 1024);
      } else if constexpr (n < 24) {
        glds16(qbase + (size_t)(n - 18) * 96 * 3456, h ? voff_q1 : voff_q0, sb + so + (jq + h) * 1024);
      } else {
        glds16((h ? ubase : gbase) + ((size_t)(n - 24) * 192 * 9216 + 8192 + 32 * c), voff_gq, sb + so + h * 6144 + jg * 1024);
      }
    } else if constexpr (K < ndma_at(I) + nx_at(I)) {
      constexpr int J = I + DX, e = K - ndma_at(I), ks = 2 * J + (e >> 1), m = e & 1;
      gload16(xf[ks][m], xbase + (m * 18 + ks) * 1024, l16);
    } else if constexpr (K < nld_at(I)) {
      constexpr int e = K - ndma_at(I) - nx_at(I);
      const char* hb = ws + WS_XF + (size_t)(r > 0 ? r * 16 - 1 : 0) * 18 * 1024;
      const int j = e < 2 ? w + 8 * e : (w < 2 ? w + 16 : 17);
      glds16(hb + j * 1024, l16, sb + HALO + j * 1024);
    }
  };
  auto ld_all = [&](auto ii) { sfor<0, nld_at(decltype(ii)::v)>([&](auto kk) { ld_op(ii, kk); }); };
  auto end_item = [&](auto ii) {
    constexpr int I = decltype(ii)::v, J = I + 1;
    if constexpr (wait_at(I) < 63) wait_vm<wait_at(I)>();
    if constexpr (J >= 0 && J < 9) { pin(xf[2 * J][0]); pin(xf[2 * J][1]); pin(xf[2 * J + 1][0]); pin(xf[2 * J + 1][1]); }
  };

  unsigned tb[4];
  auto set_big = [&](unsigned sb) { const int f = (q >> 1) | ((g & 1) << 1);
    UNROLL for (int t = 0; t < 4; ++t) { tb[t] = sb + (8 * g + q) * 128 + ((t ^ f) * 32) + 8 * p; opaque(tb[t]); } };
  auto set_qkv = [&](unsigned sb) { const int woff = (36 * c) & 8;
    UNROLL for (int t = 0; t < 4; ++t) { int G = 4 * (t ^ (w & 3)) + p; if (G > 14) G = 14;
      tb[t] = sb + (16 * (g >> 1) + 2 * (q + 4 * (g & 1))) * 144 + (G / 5) * 8 + woff + 8 * G; opaque(tb[t]); } };
  auto set_gq = [&](unsigned sb) { tb[0] = sb + (16 * (g >> 1) + 4 * (g & 1) + q) * 32 + 8 * p; opaque(tb[0]); };
  auto faddr = [&](auto nn, auto ff, auto hh) -> unsigned {
    constexpr int n = decltype(nn)::v, fi = decltype(ff)::v, i = decltype(hh)::v; constexpr unsigned so = (n % RING) * SLOT - region(n);
    if constexpr (n < 18) return tb[fi & 3] + (so + ((fi >> 2) & 1) * 8192 + (fi >> 3) * 4096 + i * 512);
    else if constexpr (n < 24) return tb[fi & 3] + (so + (fi >> 2) * 32 * 144 + i * 144);
    else return tb[0] + (so + (fi & 1) * 6144 + (fi >> 1) * 1024 + i * 256);
  };

  f32x4 acc[8][2];
  f32x4 acch;
  bf16x8 af[4];
  bf16x8 hfr;
  unsigned haddr;
  auto first3 = [&](auto nn) {
    SCHED();
    sfor<0, 3>([&](auto ff) { af[decltype(ff)::v] = cat(trd(faddr(nn, ff, IC<0>{})), trd(faddr(nn, ff, IC<1>{}))); });
    SCHED();
  };
  auto run_stage = [&](auto nn, auto ii) {
    constexpr int n = decltype(nn)::v, I = decltype(ii)::v, NF = nfrag(n);
    constexpr bool big = n < 18, qk = n >= 18 && n < 24;
    constexpr int st = n < 18 ? n % 9 : n < 24 ? n - 18 : n - 24;
    constexpr bool last = n == 8 || n == 17 || n == 23 || n == 26;
    constexpr bool has_next = !last && (n >= 24 || !late(n + 1));
    constexpr int NL = n >= 24 ? 0 : nld_at(I);
    sfor<0, NF>([&](auto ff) {
      constexpr int fi = decltype(ff)::v;
      constexpr int kstep = big ? 2 * st + (fi >> 3) : qk ? 3 * st + (fi >> 2) : 6 * st + (fi >> 1);
      constexpr int ai = big ? (fi & 7) : qk ? (fi & 3) : (fi & 1);
      constexpr bool pf = (fi + 3 < NF) || has_next;
      constexpr int pn = fi + 3 < NF ? n : n + 1, pfi = fi + 3 < NF ? fi + 3 : fi + 3 - NF;
      const bf16x8 cur = af[fi & 3];
      s16x4 lo, hi;
      mma<kstep == 0>(acc[ai][0], cur, xf[kstep][0]);
      SCHED();
      if constexpr (pf && pn != n && pfi == 0 && region(pn) != region(n)) {
        UNROLL for (int t = 0; t < (n < 24 ? 4 : 1); ++t) { tb[t] += region(pn) - region(n); opaque(tb[t]); } }
      if constexpr (pf) lo = trd(faddr(IC<pn>{}, IC<pfi>{}, IC<0>{}));
      SCHED();
      mma<kstep == 0>(acc[ai][1], cur, xf[kstep][1]);
      SCHED();
      if constexpr (pf) hi = trd(faddr(IC<pn>{}, IC<pfi>{}, IC<1>{}));
      SCHED();
      if constexpr (qk && ai == 0) {
        mma<kstep == 0>(acch, cur, hfr);
        SCHED();
        if constexpr (kstep + 1 < 18) hfr = *(LAS bf16x8*)(size_t)(haddr + (kstep + 1) * 1024);
        SCHED();
      }
      if constexpr (pf) af[(fi + 3) & 3] = cat(lo, hi);
      sfor<0, NL>([&](auto kk) { if constexpr (decltype(kk)::v * (NF - 1) / (NL > 0 ? NL : 1) == fi) { ld_op(ii, kk); SCHED(); } });
    });
    if constexpr (last) mma_drain();
    if constexpr (!last && !has_next && region(n + 1) != region(n)) { UNROLL for (int t = 0; t < 4; ++t) { tb[t] += region(n + 1) - region(n); opaque(tb[t]); } }
  };
  char* hbase = ws + WS_HF + ((size_t)((r * 8 + w) * 4 + (c >> 3)) * 36) * 2048;
  auto ep_unit = [&](auto gg, auto uu) {
    constexpr int gi = decltype(gg)::v, T = decltype(uu)::v >> 1, m = decltype(uu)::v & 1;
    const u32x2 o0 = silu_mul4(acc[2 * T][m], acc[4 + 2 * T][m]), o1 = silu_mul4(acc[2 * T + 1][m], acc[5 + 2 * T][m]);
    const u32x4 o = {o0[0], o0[1], o1[0], o1[1]};
    *(u32x4*)(hbase + (size_t)((4 * (c & 7) + 2 * gi + T) * 2 + m) * 1024 + l16) = o;
  };

  set_big(sb + region(0));
  sfor<FIRST_IT, 0>([&](auto ii) { ld_all(ii); end_item(ii); });

  auto rebase = [&](auto aa, auto bb) { constexpr unsigned d = region(decltype(bb)::v) - region(decltype(aa)::v);
    if constexpr (d != 0) { UNROLL for (int t = 0; t < 4; ++t) { tb[t] += d; opaque(tb[t]); } } };
  sfor<0, NIT>([&](auto ii) {
    constexpr int I = decltype(ii)::v; constexpr int n = stage_at(I);
    if constexpr (BAR[I]) barrier();
    STAMP(I + 1);
    if constexpr (n >= 0) {
      if constexpr (late(n)) first3(IC<n>{});
      run_stage(IC<n>{}, ii);
    } else {
      ld_all(ii);
      if constexpr (I == 9) {
        sfor<0, 4>([&](auto uu) { ep_unit(IC<0>{}, uu); });
        rebase(IC<8>{}, IC<9>{}); first3(IC<9>{});
      } else if constexpr (I == 19) {
        sfor<0, 4>([&](auto uu) { ep_unit(IC<1>{}, uu); });
        set_qkv(sb + region(18)); haddr = sb + HALO + l16; opaque(haddr); hfr = *(LAS bf16x8*)(size_t)haddr; first3(IC<18>{});
      } else if constexpr (I == 26) {
        UNROLL for (int t = 0; t < 4; ++t) {
          const int G = 4 * (t ^ (w & 3)) + g;
          const unsigned a = sb + DUMP + (16 + 32 * w + (lane & 15)) * ROWB + (G < 15 ? (G / 5) * 8 : 16) + 8 * G;
          UNROLL for (int m = 0; m < 2; ++m)
            *(LAS u32x2*)(size_t)(a + m * 16 * ROWB) = (u32x2){pk_bf16(acc[t][m][0], acc[t][m][1]), pk_bf16(acc[t][m][2], acc[t][m][3])};
          if (t == 0 && w < 4) *(LAS u32x2*)(size_t)(a - (16 + 32 * w) * ROWB) = (u32x2){pk_bf16(acch[0], acch[1]), pk_bf16(acch[2], acch[3])};
        }
        asm volatile("s_waitcnt lgkmcnt(0)" ::: "memory");
      }
    }
    end_item(ii);
  });
  auto attention = [&]() {
        const int lr = 32 * w + (lane >> 1), hf = lane & 1, grow = r * 256 + lr;
        const bool odd = c & 1;
        const unsigned ra = sb + DUMP + (lr + 2) * ROWB + hf * 16;
        const unsigned rb = sb + DUMP + (lr + 2) * ROWB + 32 + hf * 4;
        u32x4 qv = *(LAS u32x4*)(size_t)(ra + 14 * ROWB);
        unsigned q4 = *(LAS unsigned*)(size_t)(rb + 14 * ROWB);
        if (odd) qv[0] = hf ? qv[0] : 0u; else q4 = hf ? 0u : q4;
        float pw[8], mx = -3.0e38f;
        UNROLL for (int j = 0; j < 8; ++j) {
          const u32x4 kv = *(LAS u32x4*)(size_t)(ra + 48 + (14 - 2 * j) * ROWB);
          const unsigned k4 = *(LAS unsigned*)(size_t)(rb + 48 + (14 - 2 * j) * ROWB);
          float s = dot2(qv[0], kv[0], 0.f); s = dot2(qv[1], kv[1], s); s = dot2(qv[2], kv[2], s); s = dot2(qv[3], kv[3], s); s = dot2(q4, k4, s);
          s += __builtin_bit_cast(float, __builtin_amdgcn_update_dpp(0, __builtin_bit_cast(int, s), 0xB1, 0xf, 0xf, true));
          s *= 0.23570226039551584f * 1.4426950408889634f;
          s = (grow - 2 * j >= 0) ? s : -1.4426950e9f;
          pw[j] = s; mx = fmaxf(mx, s);
        }
        float sum = 0.f;
        UNROLL for (int j = 0; j < 8; ++j) { pw[j] = __builtin_amdgcn_exp2f(pw[j] - mx); sum += pw[j]; }
        const float inv = __builtin_amdgcn_rcpf(sum);
        float o[10];
        UNROLL for (int e = 0; e < 10; ++e) o[e] = 0.f;
        UNROLL for (int j = 0; j < 8; ++j) {
          const u32x4 vv = *(LAS u32x4*)(size_t)(ra + 96 + (14 - 2 * j) * ROWB);
          const unsigned v4 = *(LAS unsigned*)(size_t)(rb + 96 + (14 - 2 * j) * ROWB);
          const unsigned pl = pk_bf16(pw[j] * inv, 0.f), ph = pl << 16;
          UNROLL for (int e = 0; e < 4; ++e) { o[2 * e] = dot2(vv[e], pl, o[2 * e]); o[2 * e + 1] = dot2(vv[e], ph, o[2 * e + 1]); }
          o[8] = dot2(v4, pl, o[8]); o[9] = dot2(v4, ph, o[9]);
        }
        u32x4 ov = {pk_bf16(o[0], o[1]), pk_bf16(o[2], o[3]), pk_bf16(o[4], o[5]), pk_bf16(o[6], o[7])};
        const unsigned o4 = pk_bf16(o[8], o[9]);
        const unsigned o4x = (unsigned)__builtin_amdgcn_update_dpp(0, (int)o4, 0xB1, 0xf, 0xf, true);
        if (odd) ov[0] = hf ? ov[0] : o4x;
        char* ob = ws + WS_OF + (size_t)(grow >> 4) * 18 * 1024;
        *(u32x4*)(ob + (c >> 1) * 1024 + ((grow & 15) + 16 * (2 * (c & 1) + hf)) * 16) = ov;
        *(unsigned*)(ob + (16 + (c >> 4)) * 1024 + ((grow & 15) + 16 * ((c >> 2) & 3)) * 16 + 4 * (c & 3)) = hf ? o4x : o4;
  };
  auto quarter = [&]() {
    set_gq(sb + region(24)); first3(IC<24>{});
    sfor<24, 27>([&](auto nn) { run_stage(nn, IC<NIT>{}); });
    UNROLL for (int m = 0; m < 2; ++m)
      *(u32x2*)(hbase + (size_t)((32 + ((c & 7) >> 1)) * 2 + m) * 1024 + l16 + (c & 1) * 8) = silu_mul4(acc[0][m], acc[1][m]);
  };
  barrier();
  STAMP(28);
  if (w < 4) { attention(); SCHED(); STAMP(29); quarter(); } else { quarter(); SCHED(); STAMP(29); attention(); }
  STAMP(30);
  STAMP(31); STAMP_RT(63); STAMP_OUT(0);
}

namespace k3 {
constexpr int STG = 36864, RING = 4, LDS_BYTES = RING * STG;
constexpr int HD = 3;
constexpr int WI[9] = {-2, -1, 0, 0, 1, 2, 3, 4, 5};
constexpr int HI[9] = {-2, -1, 0, 0, 1, 2, 3, 4, 5};
constexpr int nld_at(int i) { int c = 0; for (int n = 0; n < 9; ++n) { if (WI[n] == i) c += 5; if (HI[n] == i) c += 4; } return c; }
constexpr int cum_end(int i) { int c = 0; for (int q = -2; q <= i; ++q) c += nld_at(q); return c; }
constexpr int op_code(int i, int k) { int c = 0; for (int n = 0; n < 9; ++n) { if (WI[n] == i) { if (k < c + 5) return n * 16 + (k - c); c += 5; } if (HI[n] == i) { if (k < c + 4) return n * 16 + 8 + (k - c); c += 4; } } return -1; }
constexpr int op_index(int code, int i) { for (int k = 0; k < nld_at(i); ++k) if (op_code(i, k) == code) return cum_end(i - 1) + k + 1; return 0; }
constexpr int wait_at(int i) {
  int need = 0;
  if (i + 1 < 9) { int o = op_index((i + 1) * 16 + 11, HI[i + 1]); if (o > need) need = o; o = op_index((i + 1) * 16 + 4, WI[i + 1]); if (o > need) need = o; }
  if (i + 2 < 9 && i >= 0) { int o = op_index((i + 2) * 16 + 4, WI[i + 2]); if (o > need) need = o; }
  return need == 0 ? 63 : cum_end(i) - need; }
}
__global__ __launch_bounds__(512, 2) void k3_kernel(const u16* __restrict__ x, u16* __restrict__ out, char* __restrict__ ws) {
  using namespace k3;
  extern __shared__ __attribute__((aligned(16))) char shm[];
  const unsigned sb = (unsigned)(size_t)(LAS char*)shm;
  const int tid = threadIdx.x, lane = tid & 63, w = __builtin_amdgcn_readfirstlane(tid >> 6), b = blockIdx.x;
  const int xcd = b & 7, bi = b >> 3, ks = xcd & 3, rt = (xcd >> 2) * 8 + (bi >> 2), ct = bi & 3;
  const int wm = w & 3, kh = w >> 2;
  STAMP_DECL; STAMP_RT(62); STAMP(0);
  const unsigned l16 = lane * 16;
  const char* wdb = ws + WS_WD + (size_t)(ct * 4 + ks) * 36 * 9 * 1024;
  const char* hb = ws + WS_HF + ((size_t)((rt * 4 + wm) * 4 + ks) * 36 + 2 * kh) * 2048;
  bf16x8 hf[HD + 1][4];
  auto ld_op = [&](auto ii, auto kk) {
    constexpr int code = op_code(decltype(ii)::v, decltype(kk)::v), n = code >> 4, j = code & 15; constexpr unsigned so = (n % RING) * STG;
    if constexpr (j < 4) glds16(wdb + ((size_t)n * STG + (j * 8 + w) * 1024), l16, sb + so + (j * 8 + w) * 1024);
    else if constexpr (j == 4) glds16_half(wdb + ((size_t)n * STG + 32768 + w * 512), l16, sb + so + 32768 + w * 512);
    else gload16(hf[n % (HD + 1)][j - 8], hb + ((size_t)n * 8192 + (j - 8) * 1024), l16);
  };
  f32x4 acc[9][2];
  bf16x8 af[4];
  const f32x4 zero4 = {0.f, 0.f, 0.f, 0.f};
  unsigned ab0 = sb + kh * 18432 + l16, ab1 = ab0 + 2 * STG; opaque(ab0); opaque(ab1);
  auto ard = [&](auto nn, auto ff) -> bf16x8 { constexpr int n = decltype(nn)::v, fi = decltype(ff)::v; return *(LAS bf16x8*)(size_t)(((n % RING) >= 2 ? ab1 : ab0) + ((n % 2) * STG + fi * 1024)); };
  sfor<-2, 0>([&](auto ii) { sfor<0, nld_at(decltype(ii)::v)>([&](auto kk) { ld_op(ii, kk); }); });
  wait_vm<wait_at(-1)>();
  sfor<0, 9>([&](auto nn) {
    constexpr int n = decltype(nn)::v, NL = nld_at(n);
    barrier();
    STAMP(n + 1);
    UNROLL for (int j = 0; j < 4; ++j) pin(hf[n % (HD + 1)][j]);
    if constexpr (n <= 1) { sfor<0, 3>([&](auto ff) { af[(n * 18 + decltype(ff)::v) & 3] = ard(nn, ff); }); SCHED(); }
    sfor<0, 18>([&](auto ff) {
      constexpr int fi = decltype(ff)::v, gf = n * 18 + fi, sub = fi / 9, nt = fi % 9;
      constexpr bool pf = (fi + 3 < 18) || (n < 8 && n >= 1);
      constexpr int pn = fi + 3 < 18 ? n : n + 1, pfi = fi + 3 < 18 ? fi + 3 : fi + 3 - 18;
      const bf16x8 cur = af[gf & 3];
      mma<(n == 0 && sub == 0)>(acc[nt][0], cur, hf[n % (HD + 1)][2 * sub]);
      SCHED();
      if constexpr (pf) af[(gf + 3) & 3] = ard(IC<pn>{}, IC<pfi>{});
      SCHED();
      mma<(n == 0 && sub == 0)>(acc[nt][1], cur, hf[n % (HD + 1)][2 * sub + 1]);
      SCHED();
      sfor<0, NL>([&](auto kk) { if constexpr (decltype(kk)::v * 17 / (NL > 0 ? NL : 1) == fi) { ld_op(nn, kk); SCHED(); } });
    });
    if constexpr (wait_at(n) < 63) wait_vm<wait_at(n)>();
  });
  mma_drain();
  barrier();
  STAMP(10);
  const char* ob = ws + WS_OF + (size_t)(rt * 8 + ks * 2) * 18 * 1024;
  UNROLL for (int j = 0; j < 4; ++j) glds16(ob + (j * 8 + w) * 1024, l16, sb + 73728 + (j * 8 + w) * 1024);
  glds16_half(ob + 32768 + w * 512, l16, sb + 73728 + 32768 + w * 512);
  UNROLL for (int nt = 0; nt < 9; ++nt) *(LAS f32x4*)(size_t)(sb + (w * 9 + nt) * 1024 + l16) = kh ? acc[nt][0] : acc[nt][1];
  asm volatile("s_waitcnt lgkmcnt(0)" ::: "memory");
  barrier();
  STAMP(11);
  f32x4 part[9];
  UNROLL for (int nt = 0; nt < 9; ++nt) part[nt] = (kh ? acc[nt][1] : acc[nt][0]) + *(LAS f32x4*)(size_t)(sb + ((w ^ 4) * 9 + nt) * 1024 + l16);
  const int tile = rt * 4 + ct;
  const __amdgpu_buffer_rsrc_t prs = __builtin_amdgcn_make_buffer_rsrc((void*)(ws + WS_PART + (size_t)tile * 16 * 18 * 1024), 0, 16 * 18 * 1024, 0x00020000);
  GAS unsigned* flags = (GAS unsigned*)(ws + WS_FLAG);
  if (wm != ks) {
    UNROLL for (int nt = 0; nt < 9; ++nt)
      __builtin_amdgcn_raw_buffer_store_b128(__builtin_bit_cast(u32x4, part[nt]), prs, l16 + ((wm * 4 + ks) * 18 + kh * 9 + nt) * 1024, 0, 16);
    asm volatile("s_waitcnt vmcnt(0)" ::: "memory");
    if (lane == 0) __hip_atomic_fetch_add(flags + (tile * 4 + wm) * 32, 1u, __ATOMIC_RELAXED, __HIP_MEMORY_SCOPE_AGENT);
  } else {
    UNROLL for (int nt = 0; nt < 9; ++nt) *(LAS f32x4*)(size_t)(sb + 110592 + (kh * 9 + nt) * 1024 + l16) = part[nt];
    asm volatile("s_waitcnt vmcnt(0) lgkmcnt(0)" ::: "memory");
  }
  STAMP(12);
  barrier();
  STAMP(13);
  f32x4 s0 = zero4, s1 = zero4, s2 = zero4;
  {
    const char* wob = ws + WS_WO + (size_t)(ct * 9 + w) * 18 * 1024;
    bf16x8 wf[18];
    UNROLL for (int s = 0; s < 18; ++s) wf[s] = *(const bf16x8*)(wob + s * 1024 + l16);
    UNROLL for (int s = 0; s < 18; ++s) {
      s0 = mfma(wf[s], *(LAS bf16x8*)(size_t)(sb + 73728 + s * 1024 + l16), s0);
      s1 = mfma(wf[s], *(LAS bf16x8*)(size_t)(sb + 73728 + (18 + s) * 1024 + l16), s1);
    }
    if (wm == ks) {
      const char* wo8 = ws + WS_WO + (size_t)(ct * 9 + 8) * 18 * 1024;
      UNROLL for (int s = 0; s < 18; ++s) wf[s] = *(const bf16x8*)(wo8 + s * 1024 + l16);
      UNROLL for (int s = 0; s < 18; ++s) s2 = mfma(wf[s], *(LAS bf16x8*)(size_t)(sb + 73728 + (kh * 18 + s) * 1024 + l16), s2);
    }
  }
  const size_t e0 = (size_t)(rt * 128 + ks * 32 + (lane & 15)) * 576 + ct * 144 + 4 * (lane >> 4);
  const size_t ea = e0 + w * 16, eb = ea + 16 * 576, ec = e0 + 128 + kh * 16 * 576;
  const u32x2 x0 = *(const u32x2*)(x + ea), x1 = *(const u32x2*)(x + eb), x2 = *(const u32x2*)(x + ec);
  STAMP(14);
  if (w == 0) {
    unsigned spins = 0;
    while (__builtin_amdgcn_readfirstlane(__hip_atomic_load(flags + (tile * 4 + ks) * 32, __ATOMIC_RELAXED, __HIP_MEMORY_SCOPE_AGENT)) < 6u) {
      __builtin_amdgcn_s_sleep(1);
      if (++spins > 2000000u) break;
    }
  }
  STAMP(15);
  barrier();
  STAMP(16);
  auto ysum = [&](int blk) -> f32x4 {
    f32x4 y = *(LAS f32x4*)(size_t)(sb + 110592 + blk * 1024 + l16);
    UNROLL for (int j = 1; j < 4; ++j)
      y += __builtin_bit_cast(f32x4, __builtin_amdgcn_raw_buffer_load_b128(prs, l16 + ((ks * 4 + ((ks + j) & 3)) * 18 + blk) * 1024, 0, 16));
    return y; };
  auto fin = [&](f32x4 y, f32x4 s, u32x2 xv) -> u32x2 {
    u32x2 o = {pk_bf16(bf_lo(xv[0]) + silu_mul(y[0], s[0]), bf_hi(xv[0]) + silu_mul(y[1], s[1])),
               pk_bf16(bf_lo(xv[1]) + silu_mul(y[2], s[2]), bf_hi(xv[1]) + silu_mul(y[3], s[3]))};
    return o; };
  const f32x4 y0 = ysum(w), y1 = ysum(9 + w), y2 = ysum(kh * 9 + 8);
  *(u32x2*)(out + ea) = fin(y0, s0, x0);
  *(u32x2*)(out + eb) = fin(y1, s1, x1);
  if (wm == ks) *(u32x2*)(out + ec) = fin(y2, s2, x2);
  STAMP(17); STAMP(18);
  STAMP(19); STAMP_RT(63); STAMP_OUT(1);
}

extern "C" void kernel_launch(void* const* d_in, const int* in_sizes, int n_in, void* d_out, int out_size,
                              void* d_ws, size_t ws_size, hipStream_t stream) {
  const u16* x = (const u16*)d_in[0]; const u16* wqkv = (const u16*)d_in[1]; const u16* wo = (const u16*)d_in[2];
  const u16* wg = (const u16*)d_in[4]; const u16* wu = (const u16*)d_in[5]; const u16* wd = (const u16*)d_in[6];
  char* ws = (char*)d_ws;
  (void)hipFuncSetAttribute((const void*)k1_kernel, hipFuncAttributeMaxDynamicSharedMemorySize, k1::LDS_BYTES);
  (void)hipFuncSetAttribute((const void*)k3_kernel, hipFuncAttributeMaxDynamicSharedMemorySize, k3::LDS_BYTES);
  p0_kernel<<<256, 512, 0, stream>>>(x, wo, wd, ws);
  k1_kernel<<<256, 512, k1::LDS_BYTES, stream>>>(wqkv, wg, wu, ws);
  k3_kernel<<<256, 512, k3::LDS_BYTES, stream>>>(x, (u16*)d_out, ws);
}
```

```cpp
#include <hip/hip_runtime.h>
#include <hip/hip_bf16.h>
#include <stdint.h>

typedef unsigned short u16;
typedef __attribute__((ext_vector_type(8))) __bf16 bf16x8;
typedef __attribute__((ext_vector_type(2))) __bf16 bf16x2;
typedef __attribute__((ext_vector_type(4))) short s16x4;
typedef __attribute__((ext_vector_type(4))) float f32x4;
typedef __attribute__((ext_vector_type(2))) float f32x2;
typedef __attribute__((ext_vector_type(4))) unsigned u32x4;
typedef __attribute__((ext_vector_type(2))) unsigned u32x2;
#define LAS __attribute__((address_space(3)))
#define GAS __attribute__((address_space(1)))
#define DI __device__ __forceinline__
#define UNROLL _Pragma("unroll")
#define SCHED() __builtin_amdgcn_sched_barrier(0)

template <int I> struct IC { static constexpr int v = I; };
template <int B, int E, class Fn> DI void sfor(Fn&& f) { if constexpr (B < E) { f(IC<B>{}); sfor<B + 1, E>(f); } }

constexpr int S_ = 2048, D_ = 576, F_ = 4608;
constexpr size_t WS_XF = 0;
constexpr size_t WS_HF = WS_XF + (size_t)128 * 18 * 1024;
constexpr size_t WS_OF = WS_HF + (size_t)64 * 144 * 2048;
constexpr size_t WS_WD = WS_OF + (size_t)128 * 20 * 1024;
constexpr size_t WS_WO = WS_WD + (size_t)4 * 144 * 9 * 1024;
constexpr size_t WS_PART = WS_WO + (size_t)36 * 20 * 1024;
constexpr size_t WS_FLAG = WS_PART + (size_t)256 * 57344;

DI float bf_lo(unsigned u) { return __uint_as_float(u << 16); }
DI float bf_hi(unsigned u) { return __uint_as_float(u & 0xffff0000u); }
DI unsigned pk_bf16(float a, float b) { f32x2 v = {a, b}; bf16x2 r = __builtin_convertvector(v, bf16x2); return __builtin_bit_cast(unsigned, r); }
DI float rbf(float a) { return bf_lo(pk_bf16(a, a)); }
DI float dot2(unsigned a, unsigned b, float c) { return __builtin_amdgcn_fdot2_f32_bf16(__builtin_bit_cast(bf16x2, a), __builtin_bit_cast(bf16x2, b), c, false); }
DI f32x4 mfma(bf16x8 a, bf16x8 b, f32x4 c) { return __builtin_amdgcn_mfma_f32_16x16x32_bf16(a, b, c, 0, 0, 0); }
template <bool FIRST> DI void mma(f32x4& c, const bf16x8& a, const bf16x8& b) {
  if constexpr (FIRST) asm volatile("v_mfma_f32_16x16x32_bf16 %0, %1, %2, 0" : "=&v"(c) : "v"(a), "v"(b));
  else asm volatile("v_mfma_f32_16x16x32_bf16 %0, %1, %2, %0" : "+v"(c) : "v"(a), "v"(b));
}
DI void mma_drain() { SCHED(); asm volatile("s_nop 7\n\ts_nop 1"); SCHED(); }
DI s16x4 trd(unsigned addr) { return __builtin_amdgcn_ds_read_tr16_b64_v4i16((LAS s16x4*)(size_t)addr); }
DI bf16x8 cat(s16x4 lo, s16x4 hi) { return __builtin_bit_cast(bf16x8, __builtin_shufflevector(lo, hi, 0, 1, 2, 3, 4, 5, 6, 7)); }
DI void glds16(const char* base, unsigned voff, unsigned lds) {
  asm volatile("s_mov_b32 m0, %2\n\ts_nop 0\n\tglobal_load_lds_dwordx4 %0, %1" ::"v"(voff), "s"(base), "s"(lds) : "memory");
}
DI void glds4(const char* base, unsigned voff, unsigned lds) {
  asm volatile("s_mov_b32 m0, %2\n\ts_nop 0\n\tglobal_load_lds_dword %0, %1" ::"v"(voff), "s"(base), "s"(lds) : "memory");
}
DI void glds16_half(const char* base, unsigned voff, unsigned lds) {
  asm volatile("s_mov_b32 m0, %2\n\ts_mov_b32 exec_hi, 0\n\tglobal_load_lds_dwordx4 %0, %1\n\ts_mov_b32 exec_hi, -1" ::"v"(voff), "s"(base), "s"(lds) : "memory");
}
DI void glds16_sc1(const char* base, unsigned voff, unsigned lds) {
  asm volatile("s_mov_b32 m0, %2\n\ts_nop 0\n\tglobal_load_lds_dwordx4 %0, %1 sc1" ::"v"(voff), "s"(base), "s"(lds) : "memory");
}
DI void glds16_half_sc1(const char* base, unsigned voff, unsigned lds) {
  asm volatile("s_mov_b32 m0, %2\n\ts_mov_b32 exec_hi, 0\n\tglobal_load_lds_dwordx4 %0, %1 sc1\n\ts_mov_b32 exec_hi, -1" ::"v"(voff), "s"(base), "s"(lds) : "memory");
}
DI void gstore16_sc1(char* base, unsigned voff, u32x4 v) { asm volatile("global_store_dwordx4 %0, %1, %2 sc1\n\ts_nop 1" ::"v"(voff), "v"(v), "s"(base) : "memory"); }
DI void gstore8_sc1(char* base, unsigned voff, u32x2 v) { asm volatile("global_store_dwordx2 %0, %1, %2 sc1\n\ts_nop 0" ::"v"(voff), "v"(v), "s"(base) : "memory"); }
DI void opaque(unsigned& v) { asm volatile("" : "+v"(v)); }
template <int N> DI void wait_vm() { asm volatile("s_waitcnt vmcnt(%0)" ::"n"(N > 63 ? 63 : N) : "memory"); }
DI void barrier() { SCHED(); asm volatile("" ::: "memory"); __builtin_amdgcn_s_barrier(); asm volatile("" ::: "memory"); SCHED(); }
DI void gload16(bf16x8& dst, const char* base, unsigned voff) {
  asm volatile("global_load_dwordx4 %0, %1, %2" : "=v"(dst) : "v"(voff), "s"(base) : "memory");
}
template <class T> DI void gload16_sc1(T& dst, const char* base, unsigned voff) {
  asm volatile("global_load_dwordx4 %0, %1, %2 sc1" : "=v"(dst) : "v"(voff), "s"(base) : "memory");
}
DI void pin(u32x4& v) { asm volatile("" : "+v"(v)); }
DI void pin(bf16x8& v) { asm volatile("" : "+v"(v)); }
#define STAMP_DECL
#define STAMP(i)
#define STAMP_RT(i)
#define STAMP_OUT(k)
DI float silu_mul(float g, float u) {
  float e = __builtin_amdgcn_exp2f(g * -1.4426950408889634f);
  return g * u * __builtin_amdgcn_rcpf(1.0f + e);
}

DI u32x4 silu_mul8(const f32x4 ga, const f32x4 ua, const f32x4 gb, const f32x4 ub) {
  float u0 = ua[0], u1 = ua[1], u2 = ua[2], u3 = ua[3], u4 = ub[0], u5 = ub[1], u6 = ub[2], u7 = ub[3];
  float e0, e1, e2, e3, e4, e5, e6, e7;
  asm("v_mul_f32 %0, 0xbfb8aa3b, %16\n\tv_mul_f32 %1, 0xbfb8aa3b, %17\n\tv_mul_f32 %2, 0xbfb8aa3b, %18\n\tv_mul_f32 %3, 0xbfb8aa3b, %19\n\t"
      "v_mul_f32 %4, 0xbfb8aa3b, %20\n\tv_mul_f32 %5, 0xbfb8aa3b, %21\n\tv_mul_f32 %6, 0xbfb8aa3b, %22\n\tv_mul_f32 %7, 0xbfb8aa3b, %23\n\t"
      "v_exp_f32 %0, %0\n\tv_exp_f32 %1, %1\n\tv_exp_f32 %2, %2\n\tv_exp_f32 %3, %3\n\tv_exp_f32 %4, %4\n\tv_exp_f32 %5, %5\n\tv_exp_f32 %6, %6\n\tv_exp_f32 %7, %7\n\t"
      "v_mul_f32 %8, %16, %8\n\tv_mul_f32 %9, %17, %9\n\tv_mul_f32 %10, %18, %10\n\tv_mul_f32 %11, %19, %11\n\t"
      "v_mul_f32 %12, %20, %12\n\tv_mul_f32 %13, %21, %13\n\tv_mul_f32 %14, %22, %14\n\tv_mul_f32 %15, %23, %15\n\t"
      "v_add_f32 %0, 1.0, %0\n\tv_add_f32 %1, 1.0, %1\n\tv_add_f32 %2, 1.0, %2\n\tv_add_f32 %3, 1.0, %3\n\t"
      "v_add_f32 %4, 1.0, %4\n\tv_add_f32 %5, 1.0, %5\n\tv_add_f32 %6, 1.0, %6\n\tv_add_f32 %7, 1.0, %7\n\t"
      "v_rcp_f32 %0, %0\n\tv_rcp_f32 %1, %1\n\tv_rcp_f32 %2, %2\n\tv_rcp_f32 %3, %3\n\tv_rcp_f32 %4, %4\n\tv_rcp_f32 %5, %5\n\tv_rcp_f32 %6, %6\n\tv_rcp_f32 %7, %7\n\t"
      "v_mul_f32 %8, %8, %0\n\tv_mul_f32 %9, %9, %1\n\tv_mul_f32 %10, %10, %2\n\tv_mul_f32 %11, %11, %3\n\t"
      "v_mul_f32 %12, %12, %4\n\tv_mul_f32 %13, %13, %5\n\tv_mul_f32 %14, %14, %6\n\tv_mul_f32 %15, %15, %7"
      : "=&v"(e0), "=&v"(e1), "=&v"(e2), "=&v"(e3), "=&v"(e4), "=&v"(e5), "=&v"(e6), "=&v"(e7),
        "+v"(u0), "+v"(u1), "+v"(u2), "+v"(u3), "+v"(u4), "+v"(u5), "+v"(u6), "+v"(u7)
      : "v"(ga[0]), "v"(ga[1]), "v"(ga[2]), "v"(ga[3]), "v"(gb[0]), "v"(gb[1]), "v"(gb[2]), "v"(gb[3]));
  return (u32x4){pk_bf16(u0, u1), pk_bf16(u2, u3), pk_bf16(u4, u5), pk_bf16(u6, u7)};
}
DI float xor32_max(float v) { auto r = __builtin_amdgcn_permlane32_swap(__float_as_uint(v), __float_as_uint(v), false, false); return fmaxf(__uint_as_float(r[0]), __uint_as_float(r[1])); }
DI float xor16_max(float v) { auto r = __builtin_amdgcn_permlane16_swap(__float_as_uint(v), __float_as_uint(v), false, false); return fmaxf(__uint_as_float(r[0]), __uint_as_float(r[1])); }
DI float xor32_add(float v) { auto r = __builtin_amdgcn_permlane32_swap(__float_as_uint(v), __float_as_uint(v), false, false); return __uint_as_float(r[0]) + __uint_as_float(r[1]); }
DI float xor16_add(float v) { auto r = __builtin_amdgcn_permlane16_swap(__float_as_uint(v), __float_as_uint(v), false, false); return __uint_as_float(r[0]) + __uint_as_float(r[1]); }

__global__ __launch_bounds__(512) void p0_kernel(const u16* __restrict__ x, char* __restrict__ ws) {
  const int tid = threadIdx.x, lane = tid & 63, row = blockIdx.x * 8 + (tid >> 6);
  if (tid < 3) *(unsigned*)(ws + WS_FLAG + tid * 1024 + (size_t)blockIdx.x * 4) = 0u;
  const bool act = lane < 36;
  const int l = act ? lane : 0;
  u32x4 v[2];
  UNROLL for (int j = 0; j < 2; ++j) v[j] = *(const u32x4*)((const char*)x + (size_t)row * 1152 + (l + 36 * j) * 16);
  float s = 0.f;
  UNROLL for (int j = 0; j < 2; ++j) UNROLL for (int e = 0; e < 4; ++e) s = dot2(v[j][e], 0x3f803f80u, s);
  s = act ? s : 0.f;
  UNROLL for (int o = 1; o < 16; o <<= 1) s += __shfl_xor(s, o);
  s = xor32_add(xor16_add(s));
  const float mean = s * (1.0f / 576.0f);
  float q = 0.f;
  UNROLL for (int j = 0; j < 2; ++j) UNROLL for (int e = 0; e < 4; ++e) { float a = bf_lo(v[j][e]) - mean, c = bf_hi(v[j][e]) - mean; q += a * a + c * c; }
  q = act ? q : 0.f;
  UNROLL for (int o = 1; o < 16; o <<= 1) q += __shfl_xor(q, o);
  q = xor32_add(xor16_add(q));
  const float mb = rbf(mean), vb = rbf(q * (1.0f / 576.0f));
  const float rs = 1.0f / rbf(sqrtf(rbf(vb + 1e-5f)));
  UNROLL for (int j = 0; j < 2; ++j) {
    u32x4 o;
    UNROLL for (int e = 0; e < 4; ++e) { unsigned d = pk_bf16(bf_lo(v[j][e]) - mb, bf_hi(v[j][e]) - mb); o[e] = pk_bf16(bf_lo(d) * rs, bf_hi(d) * rs); }
    const int ch = l + 36 * j, ks = ch >> 2, g = ch & 3;
    if (act) *(u32x4*)(ws + WS_XF + ((size_t)(row >> 4) * 18 + ks) * 1024 + ((row & 15) + 16 * g) * 16) = o;
  }
}

namespace pg {
constexpr int RING = 7, SLOT = 16384, LDS_BYTES = 163840;
constexpr int NST = 27;
constexpr int ROWB = 144;
constexpr int DUMP = 6 * SLOT, HALO = LDS_BYTES - 18432, SJ = RING * SLOT;
static_assert(DUMP + 272 * ROWB + 16 <= HALO, "aux");
constexpr int CSTG = 36864;
constexpr int C_O = CSTG, C_W8 = C_O + 40960, C_OWN = 3 * CSTG;
static_assert(C_W8 + 20480 <= C_OWN, "tail areas");
constexpr int FIRST_IT = -4, IT_C = 31, NIT = 40;
constexpr int item(int n) { return n < 9 ? n : n < 18 ? n + 1 : n < 24 ? n + 2 : n + 3; }
constexpr int stage_at(int i) { return i < 0 ? -1 : i < 9 ? i : i == 9 ? -1 : i < 19 ? i - 1 : i == 19 ? -1 : i < 26 ? i - 2 : i == 26 ? -1 : i < 30 ? i - 3 : -1; }
constexpr int BAR[NIT] =          {1, 1, 1, 1, 1, 1, 1, 1, 0, 0, 0, 0, 1, 1, 1, 1, 1, 1, 0, 0, 0, 0, 1, 0, 1, 0, 0, 1, 0, 0, 0, 1, 1, 1, 1, 1, 1, 1, 1, 1};
constexpr int ISSUE[NST] =        {-4, -3, -2, -1, 0, 1, 2, 3, 4, 4, 5, 6, 7, 8, 12, 12, 13, 13, 14, 14, 15, 16, 17, 22, 23, 24, 25};
constexpr bool late(int n) { return n == 0 || n == 11 || n == 20 || n == 24; }
constexpr int free_item(int n) { if (n < RING) return -100; int i = item(n - RING) + 1; while (!BAR[i]) ++i; return i; }
constexpr int need_bar(int n) { int b = late(n) ? item(n) : item(n) - 1; while (!BAR[b]) --b; return b; }
constexpr int CWI[9] = {27, 31, 31, 32, 32, 33, 34, 35, 36};
constexpr int CHI[9] = {28, 29, 29, 32, 33, 34, 35, 36, 37};
constexpr bool clate(int n) { return n <= 2; }
constexpr int FILL_AT[7] = {16, 17, 18, 22, 23, 24, 25};
constexpr int SJST_AT[4] = {10, 11, 12, 13};
enum { K_W, K_X, K_HALO, K_SJ, K_FILL, K_POLL, K_CW, K_CH, K_CO, K_C8, K_WF };
struct Op { int item, kind, a, b; };
struct Prog { Op op[256]; int n; };
constexpr Prog build() {
  Prog p{}; p.n = 0;
  for (int i = FIRST_IT; i < NIT; ++i) {
    for (int n = 0; n < NST; ++n) if (ISSUE[n] == i) for (int h = 0; h < 2; ++h) p.op[p.n++] = Op{i, K_W, n, h};
    if (i + 3 >= 0 && i + 3 < 9) for (int e = 0; e < 4; ++e) p.op[p.n++] = Op{i, K_X, i + 3, e};
    if (i == 15) for (int e = 0; e < 3; ++e) p.op[p.n++] = Op{i, K_HALO, e, 0};
    if (i == 6 || i == 7) for (int e = 0; e < 2; ++e) p.op[p.n++] = Op{i, K_SJ, (i - 6) * 2 + e, 0};
    for (int e = 0; e < 7; ++e) if (FILL_AT[e] == i) p.op[p.n++] = Op{i, K_FILL, e, 0};
    for (int n = 0; n < 9; ++n) { if (CWI[n] == i) for (int j = 0; j < 5; ++j) p.op[p.n++] = Op{i, K_CW, n, j};
                                  if (CHI[n] == i) for (int j = 0; j < 4; ++j) p.op[p.n++] = Op{i, K_CH, n, j}; }
    if (i == 37) for (int s = 0; s < 5; ++s) p.op[p.n++] = Op{i, K_WF, s, 0};
    if (i == 38) { for (int j = 0; j < 5; ++j) p.op[p.n++] = Op{i, K_CO, j, 0}; for (int j = 0; j < 3; ++j) p.op[p.n++] = Op{i, K_C8, j, 0}; for (int s = 5; s < 8; ++s) p.op[p.n++] = Op{i, K_WF, s, 0}; }
    if (i == 39) for (int s = 8; s < 20; ++s) p.op[p.n++] = Op{i, K_WF, s, 0};
  }
  return p;
}
constexpr Prog P = build();
constexpr int body_at(int i) { return i == 9 ? 4 : i == 19 ? 4 : i == 29 ? 2 : i == 30 ? 1 : 0; }
constexpr int first_op(int i) { int k = 0; while (k < P.n && P.op[k].item < i) ++k; return k; }
constexpr int nld_at(int i) { return first_op(i + 1) - first_op(i); }
constexpr int find(int kind, int a, int b) { for (int k = 0; k < P.n; ++k) if (P.op[k].kind == kind && P.op[k].a == a && P.op[k].b == b) return k; return -1; }
constexpr int bodies(int from, int upto) { int c = 0; for (int q = from + 1; q <= upto; ++q) c += body_at(q); return c; }
constexpr int after_op(int k, int upto) { return k < 0 || P.op[k].item > upto ? -1000 : first_op(upto + 1) - k - 1 + bodies(P.op[k].item, upto); }
constexpr int after_body(int t, int upto) { return first_op(upto + 1) - first_op(t + 1) + bodies(t, upto); }
constexpr int mn(int a, int b) { return a < b ? a : b; }
constexpr int FLAG_AT[4] = {0, 16, 22, 32};
constexpr int POLL_AT[4] = {0, 24, 29, 35};
constexpr int CHECK_AT[4] = {0, 26, 32, 36};
constexpr int wait_at(int i) {
  int w = 63;
  for (int n = 0; n < NST; ++n) if (need_bar(n) - 1 == i) w = mn(w, after_op(find(K_W, n, 1), i));
  if (i + 1 >= 0 && i + 1 < 9) w = mn(w, after_op(find(K_X, i + 1, 3), i));
  if (i == need_bar(18) - 1) w = mn(w, after_op(find(K_HALO, 2, 0), i));
  if (i == 8) w = mn(w, after_op(find(K_SJ, 3, 0), i));
  if (i == FLAG_AT[1] - 1) w = mn(w, after_body(SJST_AT[3], i));
  if (i == FLAG_AT[2] - 1) w = mn(w, after_body(19, i));
  if (i == FLAG_AT[3] - 1) w = mn(w, after_body(30, i));
  for (int n = 0; n < 9; ++n) { if (i == IT_C + n - 1) w = mn(w, after_op(find(K_CH, n, 3), i));
                                if (i == IT_C + n - (clate(n) ? 1 : 2)) w = mn(w, after_op(find(K_CW, n, 4), i)); }
  return w;
}
constexpr bool sched_ok() {
  for (int n = 0; n < NST; ++n) { if (ISSUE[n] < free_item(n)) return false; if (late(n) && !BAR[item(n)]) return false; }
  for (int i = FIRST_IT; i < NIT; ++i) if (wait_at(i) < 0) return false;
  for (int n = 4; n < 9; ++n) if (CWI[n] < IT_C + n - 3 || CHI[n] < IT_C + n - 3) return false;
  for (int v = 1; v < 4; ++v) if (!BAR[CHECK_AT[v] + 1] || !BAR[FLAG_AT[v]]) return false;
  if (CHI[4] <= CHECK_AT[2] || CHI[8] <= CHECK_AT[3] || CWI[0] <= CHECK_AT[1]) return false;
  return 20 % RING == 6 && free_item(27) <= 26; }
static_assert(sched_ok(), "schedule");
constexpr unsigned region(int n) { return (n % RING) * SLOT >= 65536 ? 65536u : 0u; }
constexpr int nfrag(int n) { return n < 18 ? 16 : 12; }
}

__global__ __launch_bounds__(512, 2) void main_kernel(const u16* __restrict__ x, const u16* __restrict__ wqkv, const u16* __restrict__ wo, const u16* __restrict__ wg,
                                                      const u16* __restrict__ wu, const u16* __restrict__ wd, u16* __restrict__ out, char* __restrict__ ws) {
  using namespace pg;
  extern __shared__ __attribute__((aligned(16))) char shm[];
  const unsigned sb = (unsigned)(size_t)(LAS char*)shm;
  const int tid = threadIdx.x, lane = tid & 63, w = __builtin_amdgcn_readfirstlane(tid >> 6), b = blockIdx.x;
  STAMP_DECL; STAMP_RT(62); STAMP(0);
  const int xcd = b & 7, bi = b >> 3;
  const int r = (xcd >> 2) * 4 + (bi & 3), c = (xcd & 3) * 8 + (bi >> 2);
  const int ks = xcd & 3, rt = (xcd >> 2) * 8 + (bi >> 2), ct = bi & 3, wm = w & 3, kh = w >> 2;
  const int g = lane >> 4, q = (lane & 15) >> 2, p = lane & 3;
  const unsigned l16 = lane * 16;

  unsigned voff_big, voff_q0, voff_q1, voff_gq;
  { const int row = 8 * w + (lane >> 3), pc = lane & 7, f = ((row >> 1) & 1) | (((row >> 3) & 1) << 1);
    voff_big = row * 9216 + (((pc >> 1) ^ f) * 32) + (pc & 1) * 16; }
  const int jq = w < 7 ? 2 * w : 12;
  auto qoff = [&](int j) { int n = 64 * j + lane, pos = n / 9, ch = n % 9; if (pos > 95) pos = 95;
    const int row = (pos & ~15) + 8 * ((pos >> 3) & 1) + 4 * (pos & 1) + ((pos >> 1) & 3);
    return (unsigned)(row * 3456 + (ch / 3) * 1152 + (ch % 3) * 16); };
  voff_q0 = qoff(jq); voff_q1 = qoff(jq + 1);
  const int jg = w < 6 ? w : 5;
  { const int n = 64 * jg + lane, pos = n >> 1, row = (pos & ~12) | ((pos & 4) << 1) | ((pos & 8) >> 1);
    voff_gq = row * 9216 + (n & 1) * 16; }
  const char* gbase = (const char*)wg; const char* ubase = (const char*)wu;
  const char* qbase = (const char*)wqkv + ((36 * c) & ~15);
  const char* xbase = ws + WS_XF + (size_t)(r * 16 + 2 * w) * 18 * 1024;
  int unit = b * 8 + w; if (unit > 1475) unit -= 1024;
  const bool isd = unit < 1296;
  const int su = isd ? unit : unit - 1296, kap = su / 9, lc = su % 9;
  const char* wdb = ws + WS_WD + (size_t)(ct * 4 + ks) * 36 * 9 * 1024;
  const char* hb = ws + WS_HF + ((size_t)((rt * 4 + wm) * 4 + ks) * 36 + 2 * kh) * 2048;
  const int tile = rt * 4 + ct;

  bf16x8 xf[18][2];
  bf16x8 hf[4][4];
  bf16x8 wf[20];
  u32x4 pollv;
  auto ld_op = [&](auto kk) {
    constexpr Op o = P.op[decltype(kk)::v];
    if constexpr (o.kind == K_W) {
      constexpr int n = o.a, h = o.b; constexpr unsigned so = (n % RING) * SLOT;
      if constexpr (n < 18) glds16((h ? ubase : gbase) + ((size_t)(n % 9) * 64 * 9216 + (2 * c + n / 9) * 128), voff_big, sb + so + h * 8192 + w * 1024);
      else if constexpr (n < 24) glds16(qbase + (size_t)(n - 18) * 96 * 3456, h ? voff_q1 : voff_q0, sb + so + (jq + h) * 1024);
      else glds16((h ? ubase : gbase) + ((size_t)(n - 24) * 192 * 9216 + 8192 + 32 * c), voff_gq, sb + so + h * 6144 + jg * 1024);
    } else if constexpr (o.kind == K_X) {
      constexpr int k2 = 2 * o.a + (o.b >> 1), m = o.b & 1;
      gload16(xf[k2][m], xbase + (m * 18 + k2) * 1024, l16);
    } else if constexpr (o.kind == K_HALO) {
      const int j = o.a < 2 ? w + 8 * o.a : (w < 2 ? w + 16 : 17);
      glds16(ws + WS_XF + ((size_t)(r > 0 ? r * 16 - 1 : 0) * 18 + j) * 1024, l16, sb + HALO + j * 1024);
    } else if constexpr (o.kind == K_SJ) {
      unsigned ln = lane; opaque(ln);
      const int e = ln >> 3, pc = ln & 7, gg = o.a;
      const int r0 = isd ? 32 * kap + 4 * gg : 18 * (kap < 16 ? 2 * kap : 8 * (kap - 16) + 2 * gg), rs = isd ? 16 : 18;
      const int dd = isd ? 0 : (kap < 16 ? 4 * gg : 16), par = isd ? 0 : 2;
      const int srow = r0 + rs * (e >> 2) + min(max(dd + (e & 3) - par * (e >> 2), 0), 17);
      const int f = ((e >> 1) & 1) | ((gg & 1) << 1);
      glds16((const char*)(isd ? wd : wo) + lc * 128, srow * 1152 + (((pc >> 1) ^ f) * 32) + (pc & 1) * 16, sb + SJ + w * 4096 + o.a * 1024);
    } else if constexpr (o.kind == K_FILL) {
      gstore16_sc1(ws + WS_PART + ((size_t)(tile * 4 + ks) * 56 + o.a * 8 + w) * 1024, l16, (u32x4){~0u, ~0u, ~0u, ~0u});
    } else if constexpr (o.kind == K_CW) {
      constexpr int n = o.a, j = o.b; constexpr unsigned so = (n % 4) * CSTG;
      if constexpr (j < 4) glds16_sc1(wdb + ((size_t)n * CSTG + (j * 8 + w) * 1024), l16, sb + so + (j * 8 + w) * 1024);
      else glds16_half_sc1(wdb + ((size_t)n * CSTG + 32768 + w * 512), l16, sb + so + 32768 + w * 512);
    } else if constexpr (o.kind == K_CH) {
      gload16_sc1(hf[o.a % 4][o.b], hb + ((size_t)o.a * 8192 + o.b * 1024), l16);
    } else if constexpr (o.kind == K_CO) {
      glds16_sc1(ws + WS_OF + ((size_t)(rt * 4 + ks) * 40 + o.a * 8 + w) * 1024, l16, sb + C_O + (o.a * 8 + w) * 1024);
    } else if constexpr (o.kind == K_C8) {
      const char* src = ws + WS_WO + (size_t)(ct * 9 + 8) * 20 * 1024;
      if constexpr (o.a < 2) glds16_sc1(src + (o.a * 8 + w) * 1024, l16, sb + C_W8 + (o.a * 8 + w) * 1024);
      else glds16_half_sc1(src + 16384 + w * 512, l16, sb + C_W8 + 16384 + w * 512);
    } else if constexpr (o.kind == K_WF) {
      gload16_sc1(wf[o.a], ws + WS_WO + ((size_t)(ct * 9 + w) * 20 + o.a) * 1024, l16);
    }
  };
  auto ld_all = [&](auto ii) { constexpr int I = decltype(ii)::v; sfor<first_op(I), first_op(I + 1)>([&](auto kk) { ld_op(kk); }); };
  auto ld_spread = [&](auto ii, auto ff, auto nf) {
    constexpr int I = decltype(ii)::v, fi = decltype(ff)::v, NF = decltype(nf)::v, NL = nld_at(I), F0 = first_op(I);
    sfor<0, NL>([&](auto kk) { if constexpr (decltype(kk)::v * (NF - 1) / (NL > 0 ? NL : 1) == fi) { ld_op(IC<F0 + decltype(kk)::v>{}); SCHED(); } });
  };
  auto poll_ld = [&](auto vv) {
    constexpr int V = decltype(vv)::v;
    const unsigned off = V == 1 ? l16 : (unsigned)(((lane & 8) ? ((xcd >> 2) * 4 + ct) : (rt >> 1)) * 128 + (lane & 7) * 16);
    gload16_sc1(pollv, ws + WS_FLAG + (V - 1) * 1024, off);
  };
  auto poll_rq = [&](u32x4& dst, auto vv) {
    constexpr int V = decltype(vv)::v;
    const unsigned off = V == 1 ? l16 : (unsigned)(((lane & 8) ? ((xcd >> 2) * 4 + ct) : (rt >> 1)) * 128 + (lane & 7) * 16);
    const char* fp = ws + WS_FLAG + (V - 1) * 1024;
    asm volatile("global_load_dwordx4 %0, %1, %2 sc1" : "+v"(dst) : "v"(off), "s"(fp) : "memory");
  };
  auto end_item = [&](auto ii) {
    constexpr int I = decltype(ii)::v, J = I + 1;
    if constexpr (wait_at(I) < 63) wait_vm<wait_at(I)>();
    if constexpr (J >= 0 && J < 9) { pin(xf[2 * J][0]); pin(xf[2 * J][1]); pin(xf[2 * J + 1][0]); pin(xf[2 * J + 1][1]); }
    if constexpr (J >= IT_C && J < IT_C + 9) { UNROLL for (int j = 0; j < 4; ++j) pin(hf[(J - IT_C) % 4][j]); }
    sfor<1, 4>([&](auto vv) { constexpr int V = decltype(vv)::v; if constexpr (I == CHECK_AT[V]) {
      if (w == 0) {
        wait_vm<after_body(POLL_AT[V] - 1, I) - body_at(POLL_AT[V])>();
        pin(pollv);
        unsigned spins = 0;
        while (__ballot((pollv[0] & pollv[1] & pollv[2] & pollv[3]) != 1u) != 0ull) {
          __builtin_amdgcn_s_sleep(4);
          poll_ld(vv); wait_vm<0>(); pin(pollv);
          if (++spins > 1000000u) break;
        } } } });
  };
  auto start_item = [&](auto ii) {
    constexpr int I = decltype(ii)::v;
    if constexpr (BAR[I]) barrier();
    sfor<1, 4>([&](auto vv) { constexpr int V = decltype(vv)::v;
      if constexpr (I == FLAG_AT[V]) {
        if (tid == 64) __hip_atomic_store((GAS unsigned*)(ws + WS_FLAG) + (V - 1) * 256 + r * 32 + c, 1u, __ATOMIC_RELAXED, __HIP_MEMORY_SCOPE_AGENT);
        SCHED(); }
      if constexpr (I == POLL_AT[V]) { pollv = (u32x4){0u, 0u, 0u, 0u}; pin(pollv); if (w == 0) poll_rq(pollv, vv); SCHED(); } });
    STAMP(I + 1);
  };

  unsigned tb[4];
  auto set_big = [&](unsigned sb) { const int f = (q >> 1) | ((g & 1) << 1);
    UNROLL for (int t = 0; t < 4; ++t) { tb[t] = sb + (8 * g + q) * 128 + ((t ^ f) * 32) + 8 * p; opaque(tb[t]); } };
  auto set_qkv = [&](unsigned sb) { const int woff = (36 * c) & 8;
    UNROLL for (int t = 0; t < 4; ++t) { int G = 4 * (t ^ (w & 3)) + p; if (G > 14) G = 14;
      tb[t] = sb + (16 * (g >> 1) + 2 * (q + 4 * (g & 1))) * 144 + (G / 5) * 8 + woff + 8 * G; opaque(tb[t]); } };
  auto set_gq = [&](unsigned sb) { tb[0] = sb + (16 * (g >> 1) + 4 * (g & 1) + q) * 32 + 8 * p; opaque(tb[0]); };
  auto faddr = [&](auto nn, auto ff, auto hh) -> unsigned {
    constexpr int n = decltype(nn)::v, fi = decltype(ff)::v, i = decltype(hh)::v; constexpr unsigned so = (n % RING) * SLOT - region(n);
    if constexpr (n < 18) return tb[fi & 3] + (so + ((fi >> 2) & 1) * 8192 + (fi >> 3) * 4096 + i * 512);
    else if constexpr (n < 24) return tb[fi & 3] + (so + (fi >> 2) * 32 * 144 + i * 144);
    else return tb[0] + (so + (fi & 1) * 6144 + (fi >> 1) * 1024 + i * 256);
  };

  f32x4 acc[9][2];
  f32x4 acch;
  bf16x8 af[4];
  bf16x8 hfr;
  unsigned haddr;
  const f32x4 zero4 = {0.f, 0.f, 0.f, 0.f};
  auto first3 = [&](auto nn) {
    SCHED();
    sfor<0, 3>([&](auto ff) { af[decltype(ff)::v] = cat(trd(faddr(nn, ff, IC<0>{})), trd(faddr(nn, ff, IC<1>{}))); });
    SCHED();
  };

  bf16x8 kf[3]; u32x4 qf[2]; s16x4 vt[3][2]; f32x4 sa[2], sc[2]; float s0[2], s1[2], amx[2];
  auto att_seg = [&](auto ss) {
    constexpr int SEG = decltype(ss)::v;
    const int m = lane & 15;
    const bool hi = m & 1, b0 = 4 * g + (m & 1) <= m, b1 = 4 * g + 2 + (m & 1) <= m;
    if constexpr (SEG == 0) {
      const unsigned rowa = sb + DUMP + (32 * w + m) * ROWB + (g < 3 ? g : 0) * 16;
      const unsigned tra = sb + DUMP + (32 * w + 4 * g + q) * ROWB + 96 + 8 * p;
      UNROLL for (int t = 0; t < 3; ++t) kf[t] = *(LAS bf16x8*)(size_t)(rowa + 48 + t * 16 * ROWB);
      UNROLL for (int t = 0; t < 2; ++t) qf[t] = *(LAS u32x4*)(size_t)(rowa + (t + 1) * 16 * ROWB);
      UNROLL for (int t = 0; t < 3; ++t) UNROLL for (int d = 0; d < 2; ++d) vt[t][d] = trd(tra + t * 16 * ROWB + d * 32);
    } else if constexpr (SEG == 1) {
      const bool odd = c & 1;
      const unsigned m0 = (g == 3 || (odd && g == 0)) ? 0u : ~0u, m1 = (g == 3 || (!odd && g == 2)) ? 0u : ~0u, m2 = g >= 2 ? 0u : ~0u;
      UNROLL for (int t = 0; t < 2; ++t) {
        const u32x4 qm = {qf[t][0] & m0, qf[t][1] & m1, qf[t][2] & m2, qf[t][3] & m2};
        sa[t] = mfma(kf[t], __builtin_bit_cast(bf16x8, qm), zero4); sc[t] = mfma(kf[t + 1], __builtin_bit_cast(bf16x8, qm), zero4);
      }
    } else if constexpr (SEG == 2) {
      const float K = 0.23570226039551584f * 1.4426950408889634f;
      UNROLL for (int t = 0; t < 2; ++t) {
        const float a0 = hi ? sa[t][1] : sa[t][0], a1 = hi ? sa[t][3] : sa[t][2], c0 = hi ? sc[t][1] : sc[t][0], c1 = hi ? sc[t][3] : sc[t][2];
        s0[t] = (b0 ? c0 : a0) * K; s1[t] = (b1 ? c1 : a1) * K;
        if (t == 0 && r == 0 && w == 0) { s0[t] = b0 ? s0[t] : -1.4426950e9f; s1[t] = b1 ? s1[t] : -1.4426950e9f; }
        amx[t] = xor32_max(xor16_max(fmaxf(s0[t], s1[t])));
      }
    } else if constexpr (SEG == 3) {
      UNROLL for (int t = 0; t < 2; ++t) {
        s0[t] = __builtin_amdgcn_exp2f(s0[t] - amx[t]); s1[t] = __builtin_amdgcn_exp2f(s1[t] - amx[t]);
        amx[t] = __builtin_amdgcn_rcpf(xor32_add(xor16_add(s0[t] + s1[t])));
      }
    } else if constexpr (SEG == 4) {
      UNROLL for (int t = 0; t < 2; ++t) {
        const unsigned t0 = pk_bf16(s0[t] * amx[t], 0.f) << (hi ? 16 : 0), t1 = pk_bf16(s1[t] * amx[t], 0.f) << (hi ? 16 : 0);
        const u32x4 pf = {b0 ? 0u : t0, b1 ? 0u : t1, b0 ? t0 : 0u, b1 ? t1 : 0u};
        sa[t] = mfma(cat(vt[t][0], vt[t + 1][0]), __builtin_bit_cast(bf16x8, pf), zero4);
        sc[t] = mfma(cat(vt[t][1], vt[t + 1][1]), __builtin_bit_cast(bf16x8, pf), zero4);
      }
    } else if constexpr (SEG == 5) {
      char* ob = ws + WS_OF + (size_t)(r * 8 + w) * 40960;
      gstore16_sc1(ob + c * 1024, l16, (u32x4){pk_bf16(sa[0][0], sa[0][1]), pk_bf16(sa[0][2], sa[0][3]), pk_bf16(sa[1][0], sa[1][1]), pk_bf16(sa[1][2], sa[1][3])});
      if (g == 0) gstore16_sc1(ob + 32768 + c * 256, l16, (u32x4){pk_bf16(sc[0][0], sc[0][1]), pk_bf16(sc[0][2], sc[0][3]), pk_bf16(sc[1][0], sc[1][1]), pk_bf16(sc[1][2], sc[1][3])});
    }
    SCHED();
  };

  auto side_store = [&](auto tt) {
    constexpr int t = decltype(tt)::v;
    if (b * 8 + w < 1476) {
      unsigned ln = lane; opaque(ln);
      const int gg = ln >> 4, qq = (ln & 15) >> 2, f = (qq >> 1) | ((gg & 1) << 1);
      const unsigned a = sb + SJ + w * 4096 + (8 * gg + qq) * 128 + ((t ^ f) * 32) + 8 * (ln & 3);
      u32x4 fr = __builtin_bit_cast(u32x4, cat(trd(a), trd(a + 512)));
      const unsigned z1 = (!isd && kap >= 16) ? 0u : ~0u, z2 = (!isd && kap < 16 && gg == 0) ? 0u : ~0u;
      const int k2 = kap < 128 ? kap >> 5 : (kap - 128) >> 2, step = kap < 128 ? 16 * ((kap >> 1) & 1) + 2 * ((kap >> 2) & 7) + (kap & 1) : 32 + ((kap - 128) & 3);
      const unsigned d0 = isd ? (unsigned)(WS_WD >> 10) + (k2 * 36 + step) * 9 : (unsigned)(WS_WO >> 10) + kap, dc = isd ? 4 * 36 * 9 : 180, dn = isd ? 1 : 20;
      const int NT = 4 * lc + t, c2 = NT / 9, nt = NT % 9;
      fr[1] &= z1; fr[2] &= z2;
      gstore16_sc1(ws + (size_t)(d0 + c2 * dc + nt * dn) * 1024, l16, fr);
    }
    SCHED();
  };
  auto run_stage = [&](auto nn, auto ii) {
    constexpr int n = decltype(nn)::v, NF = nfrag(n);
    constexpr bool big = n < 18, qk = n >= 18 && n < 24;
    constexpr int st = n < 18 ? n % 9 : n < 24 ? n - 18 : n - 24;
    constexpr bool last = n == 8 || n == 17 || n == 23 || n == 26;
    constexpr bool has_next = !last && !late(n + 1);
    sfor<0, NF>([&](auto ff) {
      constexpr int fi = decltype(ff)::v;
      constexpr int kstep = big ? 2 * st + (fi >> 3) : qk ? 3 * st + (fi >> 2) : 6 * st + (fi >> 1);
      constexpr int ai = big ? (fi & 7) : qk ? (fi & 3) : (fi & 1);
      constexpr bool pf = (fi + 3 < NF) || has_next;
      constexpr int pn = fi + 3 < NF ? n : n + 1, pfi = fi + 3 < NF ? fi + 3 : fi + 3 - NF;
      const bf16x8 cur = af[fi & 3];
      s16x4 lo, hi;
      mma<kstep == 0>(acc[ai][0], cur, xf[kstep][0]);
      SCHED();
      if constexpr (pf && pn != n && pfi == 0 && region(pn) != region(n)) {
        UNROLL for (int t = 0; t < (n < 24 ? 4 : 1); ++t) { tb[t] += region(pn) - region(n); opaque(tb[t]); } }
      if constexpr (pf) lo = trd(faddr(IC<pn>{}, IC<pfi>{}, IC<0>{}));
      SCHED();
      mma<kstep == 0>(acc[ai][1], cur, xf[kstep][1]);
      SCHED();
      if constexpr (pf) hi = trd(faddr(IC<pn>{}, IC<pfi>{}, IC<1>{}));
      SCHED();
      if constexpr (qk && ai == 0) {
        mma<kstep == 0>(acch, cur, hfr);
        SCHED();
        if constexpr (kstep + 1 < 18) hfr = *(LAS bf16x8*)(size_t)(haddr + (kstep + 1) * 1024);
        SCHED();
      }
      if constexpr (pf) af[(fi + 3) & 3] = cat(lo, hi);
      ld_spread(ii, ff, IC<NF>{});
      if constexpr (fi == 8) sfor<0, 4>([&](auto tt) { if constexpr (SJST_AT[decltype(tt)::v] == decltype(ii)::v) side_store(tt); });
      if constexpr (n >= 24) { constexpr int gf = (n - 24) * 12 + fi; if constexpr (gf % 5 == 0 && gf / 5 < 6) att_seg(IC<gf / 5>{}); }
    });
    if constexpr (last) mma_drain();
    if constexpr (!last && !has_next && region(n + 1) != region(n)) { UNROLL for (int t = 0; t < 4; ++t) { tb[t] += region(n + 1) - region(n); opaque(tb[t]); } }
  };
  char* hbase = ws + WS_HF + ((size_t)((r * 8 + w) * 4 + (c >> 3)) * 36) * 2048;
  auto ep_unit = [&](auto gg, auto uu) {
    constexpr int gi = decltype(gg)::v, T = decltype(uu)::v >> 1, m = decltype(uu)::v & 1;
    gstore16_sc1(hbase + ((16 * gi + 2 * (c & 7) + T) * 2 + m) * 1024, l16, silu_mul8(acc[2 * T][m], acc[4 + 2 * T][m], acc[2 * T + 1][m], acc[5 + 2 * T][m]));
  };

  set_big(sb + region(0));
  sfor<FIRST_IT, 0>([&](auto ii) { ld_all(ii); end_item(ii); });
  sfor<0, IT_C>([&](auto ii) {
    constexpr int I = decltype(ii)::v; constexpr int n = stage_at(I);
    start_item(ii);
    if constexpr (n >= 0) {
      if constexpr (n == 24) set_gq(sb + region(24));
      if constexpr (late(n)) first3(IC<n>{});
      run_stage(IC<n>{}, ii);
    } else {
      ld_all(ii);
      if constexpr (I == 9) {
        static_assert(region(8) == region(9), "bases");
        sfor<0, 4>([&](auto uu) { ep_unit(IC<0>{}, uu); });
        first3(IC<9>{});
      } else if constexpr (I == 19) {
        sfor<0, 4>([&](auto uu) { ep_unit(IC<1>{}, uu); });
        set_qkv(sb + region(18)); haddr = sb + HALO + l16; opaque(haddr); hfr = *(LAS bf16x8*)(size_t)haddr; first3(IC<18>{});
      } else if constexpr (I == 26) {
        UNROLL for (int t = 0; t < 4; ++t) {
          const int G = 4 * (t ^ (w & 3)) + g;
          const unsigned a = sb + DUMP + (16 + 32 * w + (lane & 15)) * ROWB + (G < 15 ? (G / 5) * 8 + 8 * G : 88);
          UNROLL for (int m = 0; m < 2; ++m)
            *(LAS u32x2*)(size_t)(a + m * 16 * ROWB) = (u32x2){pk_bf16(acc[t][m][0], acc[t][m][1]), pk_bf16(acc[t][m][2], acc[t][m][3])};
          if (t == 0 && w < 4) *(LAS u32x2*)(size_t)(a - (16 + 32 * w) * ROWB) = (u32x2){pk_bf16(acch[0], acch[1]), pk_bf16(acch[2], acch[3])};
        }
        asm volatile("s_waitcnt lgkmcnt(0)" ::: "memory");
      } else if constexpr (I == 30) {
        gstore16_sc1(hbase + (64 + (c & 7)) * 1024, l16, silu_mul8(acc[0][0], acc[1][0], acc[0][1], acc[1][1]));
      }
    }
    end_item(ii);
  });

  unsigned ab0 = sb + kh * 18432 + l16, ab1 = ab0 + 2 * CSTG; opaque(ab0); opaque(ab1);
  auto ard = [&](auto nn, auto ff) -> bf16x8 { constexpr int n = decltype(nn)::v, fi = decltype(ff)::v; return *(LAS bf16x8*)(size_t)(((n % 4) >= 2 ? ab1 : ab0) + ((n % 2) * CSTG + fi * 1024)); };
  sfor<0, 9>([&](auto nn) {
    constexpr int n = decltype(nn)::v, I = IT_C + n;
    start_item(IC<I>{});
    if constexpr (n == 8) {
      UNROLL for (int j = 0; j < 4; j += 2) {
        const bf16x8 a = hf[0][j], bb = hf[0][j + 1];
        hf[0][j] = __builtin_shufflevector(a, bb, 0, 1, 2, 3, 8, 9, 10, 11); hf[0][j + 1] = __builtin_shufflevector(a, bb, 4, 5, 6, 7, 12, 13, 14, 15);
        pin(hf[0][j]); pin(hf[0][j + 1]); }
      asm volatile("s_nop 1"); SCHED(); }
    if constexpr (clate(n)) { sfor<0, 3>([&](auto ff) { af[(n * 18 + decltype(ff)::v) & 3] = ard(nn, ff); }); SCHED(); }
    sfor<0, 18>([&](auto ff) {
      constexpr int fi = decltype(ff)::v, gf = n * 18 + fi, sub = fi / 9, nt = fi % 9;
      constexpr bool pf = (fi + 3 < 18) || (n < 8 && !clate(n + 1));
      constexpr int pn = fi + 3 < 18 ? n : n + 1, pfi = fi + 3 < 18 ? fi + 3 : fi + 3 - 18;
      const bf16x8 cur = af[gf & 3];
      mma<(n == 0 && sub == 0)>(acc[nt][0], cur, hf[n % 4][2 * sub]);
      SCHED();
      if constexpr (pf) af[(gf + 3) & 3] = ard(IC<pn>{}, IC<pfi>{});
      SCHED();
      mma<(n == 0 && sub == 0)>(acc[nt][1], cur, hf[n % 4][2 * sub + 1]);
      SCHED();
      ld_spread(IC<I>{}, ff, IC<18>{});
    });
    end_item(IC<I>{});
  });
  mma_drain();
  STAMP(41);
  if (wm != ks) {
    char* dst = ws + WS_PART + ((size_t)(tile * 4 + wm) * 56 + (((ks - wm - 1) & 3) * 2 + kh) * 9) * 1024;
    UNROLL for (int nt = 0; nt < 9; ++nt)
      gstore16_sc1(dst + nt * 1024, l16, (u32x4){pk_bf16(acc[nt][0][0], acc[nt][0][1]), pk_bf16(acc[nt][0][2], acc[nt][0][3]),
                                                 pk_bf16(acc[nt][1][0], acc[nt][1][1]), pk_bf16(acc[nt][1][2], acc[nt][1][3])});
    wait_vm<9>();
  } else {
    UNROLL for (int nt = 0; nt < 9; ++nt) UNROLL for (int m = 0; m < 2; ++m)
      *(LAS f32x4*)(size_t)(sb + C_OWN + ((kh * 9 + nt) * 2 + m) * 1024 + l16) = acc[nt][m];
    asm volatile("s_waitcnt vmcnt(0) lgkmcnt(0)" ::: "memory");
  }
  UNROLL for (int s = 0; s < 20; ++s) pin(wf[s]);
  STAMP(42);
  barrier();
  STAMP(43);
  const size_t e0 = (size_t)(rt * 128 + ks * 32 + (lane & 15)) * 576 + ct * 144 + 4 * g;
  const size_t ea = e0 + w * 16, eb = ea + 16 * 576, ec = e0 + 128 + kh * 16 * 576;
  const u32x2 x0 = *(const u32x2*)(x + ea), x1 = *(const u32x2*)(x + eb), x2 = *(const u32x2*)(x + ec);
  f32x4 sv0 = zero4, sv1 = zero4, sv2 = zero4;
  {
    const unsigned oa = sb + C_O + l16, ob2 = sb + C_O + 32768 + g * 512 + (lane & 15) * 16;
    auto ord = [&](int s, int half) -> u32x4 { return *(LAS u32x4*)(size_t)(s < 16 ? oa + (2 * s + half) * 1024 : ob2 + (s - 16) * 2048 + half * 256); };
    UNROLL for (int s0 = 0; s0 < 20; s0 += 5) {
      u32x4 oe[5], oo[5]; bf16x8 w8[5];
      UNROLL for (int i = 0; i < 5; ++i) { oe[i] = ord(s0 + i, 0); oo[i] = ord(s0 + i, 1); w8[i] = *(LAS bf16x8*)(size_t)(sb + C_W8 + (s0 + i) * 1024 + l16); }
      UNROLL for (int i = 0; i < 5; ++i) {
        const u32x4 f0 = {oe[i][0], oe[i][1], oo[i][0], oo[i][1]}, f1 = {oe[i][2], oe[i][3], oo[i][2], oo[i][3]};
        sv0 = mfma(wf[s0 + i], __builtin_bit_cast(bf16x8, f0), sv0);
        sv1 = mfma(wf[s0 + i], __builtin_bit_cast(bf16x8, f1), sv1);
        sv2 = mfma(w8[i], __builtin_bit_cast(bf16x8, kh ? f1 : f0), sv2);
      }
    }
  }
  f32x4 y0, y1, y2;
  { const unsigned oa = sb + C_OWN + l16;
    y0 = *(LAS f32x4*)(size_t)(oa + (w * 2) * 1024) + *(LAS f32x4*)(size_t)(oa + ((9 + w) * 2) * 1024);
    y1 = *(LAS f32x4*)(size_t)(oa + (w * 2 + 1) * 1024) + *(LAS f32x4*)(size_t)(oa + ((9 + w) * 2 + 1) * 1024);
    y2 = *(LAS f32x4*)(size_t)(oa + (16 + kh) * 1024) + *(LAS f32x4*)(size_t)(oa + (34 + kh) * 1024); }
  STAMP(44);
  const char* inb = ws + WS_PART + (size_t)(tile * 4 + ks) * 57344;
  u32x4 pa[6], pb[6];
  UNROLL for (int j = 0; j < 6; ++j) pb[j] = (u32x4){0u, 0u, 0u, 0u};
  for (unsigned spins = 0;; ++spins) {
    UNROLL for (int j = 0; j < 6; ++j) gload16_sc1(pa[j], inb + (j * 9 + w) * 1024, l16);
    if (wm == ks) { UNROLL for (int j = 0; j < 6; ++j) gload16_sc1(pb[j], inb + (j * 9 + 8) * 1024, l16);
      wait_vm<0>(); UNROLL for (int j = 0; j < 6; ++j) pin(pb[j]); }
    wait_vm<0>();
    unsigned mx = 0;
    UNROLL for (int j = 0; j < 6; ++j) { pin(pa[j]);
      mx = max(max(mx, max(pa[j][0], pa[j][1])), max(pa[j][2], pa[j][3])); mx = max(max(mx, max(pb[j][0], pb[j][1])), max(pb[j][2], pb[j][3])); }
    if (__ballot(mx == ~0u) == 0ull || spins > 200000u) break;
    __builtin_amdgcn_s_sleep(4);
  }
  STAMP(45);
  UNROLL for (int j = 0; j < 6; ++j) {
    y0 += (f32x4){bf_lo(pa[j][0]), bf_hi(pa[j][0]), bf_lo(pa[j][1]), bf_hi(pa[j][1])};
    y1 += (f32x4){bf_lo(pa[j][2]), bf_hi(pa[j][2]), bf_lo(pa[j][3]), bf_hi(pa[j][3])};
    const unsigned u0 = kh ? pb[j][2] : pb[j][0], u1 = kh ? pb[j][3] : pb[j][1];
    y2 += (f32x4){bf_lo(u0), bf_hi(u0), bf_lo(u1), bf_hi(u1)};
  }
  auto fin = [&](f32x4 y, f32x4 s, u32x2 xv) -> u32x2 {
    u32x2 o = {pk_bf16(bf_lo(xv[0]) + silu_mul(y[0], s[0]), bf_hi(xv[0]) + silu_mul(y[1], s[1])),
               pk_bf16(bf_lo(xv[1]) + silu_mul(y[2], s[2]), bf_hi(xv[1]) + silu_mul(y[3], s[3]))};
    return o; };
  *(u32x2*)(out + ea) = fin(y0, sv0, x0);
  *(u32x2*)(out + eb) = fin(y1, sv1, x1);
  if (wm == ks) *(u32x2*)(out + ec) = fin(y2, sv2, x2);
  STAMP(46);
  STAMP(47); STAMP_RT(63); STAMP_OUT(0);
}

extern "C" void kernel_launch(void* const* d_in, const int* in_sizes, int n_in, void* d_out, int out_size,
                              void* d_ws, size_t ws_size, hipStream_t stream) {
  const u16* x = (const u16*)d_in[0]; const u16* wqkv = (const u16*)d_in[1]; const u16* wo = (const u16*)d_in[2];
  const u16* wg = (const u16*)d_in[4]; const u16* wu = (const u16*)d_in[5]; const u16* wd = (const u16*)d_in[6];
  char* ws = (char*)d_ws;
  (void)hipFuncSetAttribute((const void*)main_kernel, hipFuncAttributeMaxDynamicSharedMemorySize, pg::LDS_BYTES);
  p0_kernel<<<256, 512, 0, stream>>>(x, ws);
  main_kernel<<<256, 512, pg::LDS_BYTES, stream>>>(x, wqkv, wo, wg, wu, wd, (u16*)d_out, ws);
}
```

```cpp
#include <hip/hip_runtime.h>
#include <hip/hip_bf16.h>
#include <stdint.h>

typedef unsigned short u16;
typedef __attribute__((ext_vector_type(8))) __bf16 bf16x8;
typedef __attribute__((ext_vector_type(2))) __bf16 bf16x2;
typedef __attribute__((ext_vector_type(4))) short s16x4;
typedef __attribute__((ext_vector_type(4))) float f32x4;
typedef __attribute__((ext_vector_type(2))) float f32x2;
typedef __attribute__((ext_vector_type(4))) unsigned u32x4;
typedef __attribute__((ext_vector_type(2))) unsigned u32x2;
#define LAS __attribute__((address_space(3)))
#define GAS __attribute__((address_space(1)))
#define DI __device__ __forceinline__
#define UNROLL _Pragma("unroll")
#define SCHED() __builtin_amdgcn_sched_barrier(0)

template <int I> struct IC { static constexpr int v = I; };
template <int B, int E, class Fn> DI void sfor(Fn&& f) { if constexpr (B < E) { f(IC<B>{}); sfor<B + 1, E>(f); } }

constexpr int S_ = 2048, D_ = 576, F_ = 4608;
constexpr size_t WS_XF = 0;
constexpr size_t WS_HF = WS_XF + (size_t)128 * 18 * 1024;
constexpr size_t WS_OF = WS_HF + (size_t)64 * 144 * 2048;
constexpr size_t WS_WD = WS_OF + (size_t)128 * 20 * 1024;
constexpr size_t WS_WO = WS_WD + (size_t)4 * 144 * 9 * 1024;
constexpr size_t WS_W2 = WS_WO + (size_t)36 * 20 * 1024;
constexpr size_t WS_PART = WS_W2 + (size_t)32 * 108 * 1024;
constexpr size_t WS_FLAG = WS_PART + (size_t)256 * 32768;
constexpr size_t FL_A = 6144, FL_EPOCH = FL_A + 8192;

DI float bf_lo(unsigned u) { return __uint_as_float(u << 16); }
DI float bf_hi(unsigned u) { return __uint_as_float(u & 0xffff0000u); }
DI unsigned pk_bf16(float a, float b) { f32x2 v = {a, b}; bf16x2 r = __builtin_convertvector(v, bf16x2); return __builtin_bit_cast(unsigned, r); }
DI float rbf(float a) { return bf_lo(pk_bf16(a, a)); }
DI float dot2(unsigned a, unsigned b, float c) { return __builtin_amdgcn_fdot2_f32_bf16(__builtin_bit_cast(bf16x2, a), __builtin_bit_cast(bf16x2, b), c, false); }
DI f32x4 mfma(bf16x8 a, bf16x8 b, f32x4 c) { return __builtin_amdgcn_mfma_f32_16x16x32_bf16(a, b, c, 0, 0, 0); }
template <bool FIRST> DI void mma(f32x4& c, const bf16x8& a, const bf16x8& b) {
  if constexpr (FIRST) asm volatile("v_mfma_f32_16x16x32_bf16 %0, %1, %2, 0" : "=&v"(c) : "v"(a), "v"(b));
  else asm volatile("v_mfma_f32_16x16x32_bf16 %0, %1, %2, %0" : "+v"(c) : "v"(a), "v"(b));
}
DI void mma_drain() { SCHED(); asm volatile("s_nop 7\n\ts_nop 1"); SCHED(); }
DI s16x4 trd(unsigned addr) { return __builtin_amdgcn_ds_read_tr16_b64_v4i16((LAS s16x4*)(size_t)addr); }
DI bf16x8 cat(s16x4 lo, s16x4 hi) { return __builtin_bit_cast(bf16x8, __builtin_shufflevector(lo, hi, 0, 1, 2, 3, 4, 5, 6, 7)); }
template <int OFF = 0, bool SC1 = false, bool HALF = false> DI void glds16(const char* base, unsigned voff, unsigned lds) {
  if constexpr (HALF) asm volatile("s_mov_b32 m0, %2\n\ts_mov_b32 exec_hi, 0\n\tglobal_load_lds_dwordx4 %0, %1 offset:%c3 sc1\n\ts_mov_b32 exec_hi, -1" ::"v"(voff), "s"(base), "s"(lds), "n"(OFF) : "memory");
  else if constexpr (SC1) asm volatile("s_mov_b32 m0, %2\n\ts_nop 0\n\tglobal_load_lds_dwordx4 %0, %1 offset:%c3 sc1" ::"v"(voff), "s"(base), "s"(lds), "n"(OFF) : "memory");
  else asm volatile("s_mov_b32 m0, %2\n\ts_nop 0\n\tglobal_load_lds_dwordx4 %0, %1 offset:%c3" ::"v"(voff), "s"(base), "s"(lds), "n"(OFF) : "memory");
}
template <int OFF = 0> DI void gstore16_sc1(char* base, unsigned voff, u32x4 v) { asm volatile("global_store_dwordx4 %0, %1, %2 offset:%c3 sc1\n\ts_nop 1" ::"v"(voff), "v"(v), "s"(base), "n"(OFF) : "memory"); }
DI void opaque(unsigned& v) { asm volatile("" : "+v"(v)); }
DI void opaque(float& v) { asm volatile("" : "+v"(v)); }
template <class T> DI void spin(T& v) { asm volatile("" : "+s"(v)); }
template <int N> DI void wait_vm() { asm volatile("s_waitcnt vmcnt(%0)" ::"n"(N > 63 ? 63 : N) : "memory"); }
DI void barrier() { SCHED(); asm volatile("" ::: "memory"); __builtin_amdgcn_s_barrier(); asm volatile("" ::: "memory"); SCHED(); }
template <int OFF = 0, bool SC1 = false, class T> DI void gload16(T& dst, const char* base, unsigned voff) {
  if constexpr (SC1) asm volatile("global_load_dwordx4 %0, %1, %2 offset:%c3 sc1" : "=v"(dst) : "v"(voff), "s"(base), "n"(OFF) : "memory");
  else asm volatile("global_load_dwordx4 %0, %1, %2 offset:%c3" : "=v"(dst) : "v"(voff), "s"(base), "n"(OFF) : "memory");
}
template <int OFF> DI void gload16_rw(u32x4& dst, const char* base, unsigned voff) {
  asm volatile("global_load_dwordx4 %0, %1, %2 offset:%c3 sc1" : "+v"(dst) : "v"(voff), "s"(base), "n"(OFF) : "memory");
}
DI void wait0_pin(u32x4& v) { asm volatile("s_waitcnt vmcnt(0)" : "+v"(v)::"memory"); }
DI void gload8(u32x2& dst, const char* base, unsigned voff) { asm volatile("global_load_dwordx2 %0, %1, %2" : "=v"(dst) : "v"(voff), "s"(base) : "memory"); }
DI void gtouch(unsigned& dst, const char* base, unsigned voff) { asm volatile("global_load_dword %0, %1, %2" : "+v"(dst) : "v"(voff), "s"(base) : "memory"); }
DI void pin(u32x2& v) { asm volatile("" : "+v"(v)); }
DI void pin(u32x4& v) { asm volatile("" : "+v"(v)); }
DI void pin(bf16x8& v) { asm volatile("" : "+v"(v)); }
#define STAMP_DECL
#define STAMP(i)
#define STAMP_RT(i)
#define STAMP_OUT(k)
DI float silu_mul(float g, float u) {
  float e = __builtin_amdgcn_exp2f(g * -1.4426950408889634f);
  return g * u * __builtin_amdgcn_rcpf(1.0f + e);
}

DI u32x4 silu_mul8(const f32x4 ga, const f32x4 ua, const f32x4 gb, const f32x4 ub) {
  float e0, e1, e2, e3, e4, e5, e6, e7;
  asm("v_mul_f32 %0, 0xbfb8aa3b, %8\n\tv_mul_f32 %1, 0xbfb8aa3b, %9\n\tv_mul_f32 %2, 0xbfb8aa3b, %10\n\tv_mul_f32 %3, 0xbfb8aa3b, %11\n\t"
      "v_mul_f32 %4, 0xbfb8aa3b, %12\n\tv_mul_f32 %5, 0xbfb8aa3b, %13\n\tv_mul_f32 %6, 0xbfb8aa3b, %14\n\tv_mul_f32 %7, 0xbfb8aa3b, %15\n\t"
      "v_exp_f32 %0, %0\n\tv_exp_f32 %1, %1\n\tv_exp_f32 %2, %2\n\tv_exp_f32 %3, %3\n\tv_exp_f32 %4, %4\n\tv_exp_f32 %5, %5\n\tv_exp_f32 %6, %6\n\tv_exp_f32 %7, %7\n\t"
      "v_add_f32 %0, 1.0, %0\n\tv_add_f32 %1, 1.0, %1\n\tv_add_f32 %2, 1.0, %2\n\tv_add_f32 %3, 1.0, %3\n\t"
      "v_add_f32 %4, 1.0, %4\n\tv_add_f32 %5, 1.0, %5\n\tv_add_f32 %6, 1.0, %6\n\tv_add_f32 %7, 1.0, %7\n\t"
      "v_rcp_f32 %0, %0\n\tv_rcp_f32 %1, %1\n\tv_rcp_f32 %2, %2\n\tv_rcp_f32 %3, %3\n\tv_rcp_f32 %4, %4\n\tv_rcp_f32 %5, %5\n\tv_rcp_f32 %6, %6\n\tv_rcp_f32 %7, %7\n\t"
      "v_mul_f32 %0, %8, %0\n\tv_mul_f32 %1, %9, %1\n\tv_mul_f32 %2, %10, %2\n\tv_mul_f32 %3, %11, %3\n\t"
      "v_mul_f32 %4, %12, %4\n\tv_mul_f32 %5, %13, %5\n\tv_mul_f32 %6, %14, %6\n\tv_mul_f32 %7, %15, %7\n\t"
      "v_mul_f32 %0, %16, %0\n\tv_mul_f32 %1, %17, %1\n\tv_mul_f32 %2, %18, %2\n\tv_mul_f32 %3, %19, %3\n\t"
      "v_mul_f32 %4, %20, %4\n\tv_mul_f32 %5, %21, %5\n\tv_mul_f32 %6, %22, %6\n\tv_mul_f32 %7, %23, %7"
      : "=&v"(e0), "=&v"(e1), "=&v"(e2), "=&v"(e3), "=&v"(e4), "=&v"(e5), "=&v"(e6), "=&v"(e7)
      : "v"(ga[0]), "v"(ga[1]), "v"(ga[2]), "v"(ga[3]), "v"(gb[0]), "v"(gb[1]), "v"(gb[2]), "v"(gb[3]),
        "v"(ua[0]), "v"(ua[1]), "v"(ua[2]), "v"(ua[3]), "v"(ub[0]), "v"(ub[1]), "v"(ub[2]), "v"(ub[3]));
  return (u32x4){pk_bf16(e0, e1), pk_bf16(e2, e3), pk_bf16(e4, e5), pk_bf16(e6, e7)};
}
DI u32x4 pk8(const f32x4 a, const f32x4 b) { return (u32x4){pk_bf16(a[0], a[1]), pk_bf16(a[2], a[3]), pk_bf16(b[0], b[1]), pk_bf16(b[2], b[3])}; }
DI f32x4 up_lo(const u32x4 v) { return (f32x4){bf_lo(v[0]), bf_hi(v[0]), bf_lo(v[1]), bf_hi(v[1])}; }
DI f32x4 up_hi(const u32x4 v) { return (f32x4){bf_lo(v[2]), bf_hi(v[2]), bf_lo(v[3]), bf_hi(v[3])}; }
template <int CTRL> DI float dpp_add(float v) { return v + __builtin_bit_cast(float, __builtin_amdgcn_update_dpp(0, __builtin_bit_cast(int, v), CTRL, 0xf, 0xf, true)); }
DI float vmax(float a, float b) { float r; asm("v_max_f32 %0, %1, %2" : "=v"(r) : "v"(a), "v"(b)); return r; }
DI float xor32_max(float v) { auto r = __builtin_amdgcn_permlane32_swap(__float_as_uint(v), __float_as_uint(v), false, false); return vmax(__uint_as_float(r[0]), __uint_as_float(r[1])); }
DI float xor16_max(float v) { auto r = __builtin_amdgcn_permlane16_swap(__float_as_uint(v), __float_as_uint(v), false, false); return vmax(__uint_as_float(r[0]), __uint_as_float(r[1])); }
DI float xor32_add(float v) { auto r = __builtin_amdgcn_permlane32_swap(__float_as_uint(v), __float_as_uint(v), false, false); return __uint_as_float(r[0]) + __uint_as_float(r[1]); }
DI float xor16_add(float v) { auto r = __builtin_amdgcn_permlane16_swap(__float_as_uint(v), __float_as_uint(v), false, false); return __uint_as_float(r[0]) + __uint_as_float(r[1]); }

DI float wave_sum(float v) {
  v = dpp_add<0xB1>(v); v = dpp_add<0x4E>(v); v = dpp_add<0x141>(v); v = dpp_add<0x140>(v);
  return xor32_add(xor16_add(v));
}

namespace pg {
constexpr int RING = 7, SLOT = 16384, LDS_BYTES = 163840;
constexpr int NST = 27;
constexpr int ROWB = 144;
constexpr int DUMP = RING * SLOT;
static_assert(DUMP + 272 * ROWB + 16 <= LDS_BYTES, "dump");
constexpr int A_LN = 6 * SLOT, A_S = RING * SLOT, A_Q = A_S + 32768;
static_assert(A_Q + 15360 <= LDS_BYTES, "scratch");
constexpr int CSTG = 36864;
constexpr int C_O = CSTG, C_W8 = C_O + 40960, C_X = 3 * CSTG, C_OWN = 0;
static_assert(C_W8 + 20480 <= C_X && C_X + 8 * 5120 <= LDS_BYTES, "tail areas");
constexpr int FIRST_IT = -2, IT_C = 30, NIT = 39, NPRE = 4;
constexpr int item(int n) { return n < 9 ? n : n < 18 ? n + 1 : n + 2; }
constexpr int stage_at(int i) { return i < 0 ? -1 : i < 9 ? i : i == 9 ? -1 : i < 19 ? i - 1 : i == 19 ? -1 : i < 29 ? i - 2 : -1; }
constexpr int BAR[NIT] =          {1, 1, 1, 1, 1, 1, 1, 1, 0, 0, 0, 0, 1, 1, 1, 1, 1, 1, 0, 0, 0, 0, 1, 0, 1, 0, 1, 0, 0, 0, 1, 1, 1, 1, 1, 1, 1, 1, 1};
constexpr int ISSUE[NST] =        {-2, -2, -2, -2, 0, 1, 2, 3, 4, 4, 5, 6, 7, 8, 12, 12, 13, 13, 15, 15, 16, 16, 17, 22, 22, 23, 23};
constexpr bool late(int n) { return n == 0 || n == 11 || n == 20; }
constexpr int free_item(int n) { if (n < RING) return -100; int i = item(n - RING) + 1; while (!BAR[i]) ++i; return i; }
constexpr int need_bar(int n) { int b = late(n) ? item(n) : item(n) - 1; while (!BAR[b]) --b; return b; }
constexpr int CWI[9] = {26, 30, 30, 31, 32, 33, 34, 35, 36};
constexpr int CHI[9] = {27, 28, 30, 31, 32, 33, 34, 35, 36};
constexpr int WF_AT[20] = {21, 21, 21, 21, 23, 23, 24, 24, 24, 24, 25, 25, 25, 25, 38, 38, 38, 38, 38, 38};
constexpr bool clate(int n) { return n <= 8; }
constexpr int FILL_AT[4] = {14, 14, 17, 18};
constexpr int SJ0 = 7;
constexpr int X_AT[9] = {-1, -1, 0, 0, 1, 2, 3, 4, 5};
constexpr int SJ_AT[4] = {1, 2, 3, 3};
enum { K_SJ, K_W, K_X, K_FILL, K_CW, K_CH, K_CO, K_C8, K_WF, K_XL };
struct Op { int item, kind, a, b; };
struct Prog { Op op[256]; int n; };
constexpr Prog build() {
  Prog p{}; p.n = 0;
  for (int i = FIRST_IT; i < NIT; ++i) {
    for (int j = 0; j < 9; ++j) if (X_AT[j] == i) for (int e = 0; e < 4; ++e) p.op[p.n++] = Op{i, K_X, j, e};
    for (int n = 0; n < NST; ++n) if (ISSUE[n] == i) for (int h = 0; h < 2; ++h) p.op[p.n++] = Op{i, K_W, n, h};
    for (int e = 0; e < 4; ++e) if (SJ_AT[e] == i) p.op[p.n++] = Op{i, K_SJ, e, 0};
    for (int e = 0; e < 4; ++e) if (FILL_AT[e] == i) p.op[p.n++] = Op{i, K_FILL, e, 0};
    for (int n = 0; n < 9; ++n) { if (CWI[n] == i) for (int j = 0; j < 5; ++j) p.op[p.n++] = Op{i, K_CW, n, j};
                                  if (CHI[n] == i) for (int j = 0; j < 4; ++j) p.op[p.n++] = Op{i, K_CH, n, j}; }
    if (i == 37) { for (int j = 0; j < 5; ++j) p.op[p.n++] = Op{i, K_CO, j, 0}; for (int j = 0; j < 3; ++j) p.op[p.n++] = Op{i, K_C8, j, 0}; }
    for (int s = 0; s < 20; ++s) if (WF_AT[s] == i) p.op[p.n++] = Op{i, K_WF, s, 0};
    if (i == 38) for (int j = 0; j < 3; ++j) p.op[p.n++] = Op{i, K_XL, j, 0};
  }
  return p;
}
constexpr Prog P = build();
constexpr int body_at(int i) { return i == 9 ? 4 : i == 19 ? 4 : i == 29 ? 1 : i == 31 ? 2 : 0; }
constexpr int first_op(int i) { int k = 0; while (k < P.n && P.op[k].item < i) ++k; return k; }
constexpr int nld_at(int i) { return first_op(i + 1) - first_op(i); }
constexpr int find(int kind, int a, int b) { for (int k = 0; k < P.n; ++k) if (P.op[k].kind == kind && P.op[k].a == a && P.op[k].b == b) return k; return -1; }
constexpr int bodies(int from, int upto) { int c = 0; for (int q = from + 1; q <= upto; ++q) c += body_at(q); return c; }
constexpr int after_op(int k, int upto) { return k < 0 || P.op[k].item > upto ? -1000 : first_op(upto + 1) - k - 1 + bodies(P.op[k].item, upto); }
constexpr int after_body(int t, int upto) { return first_op(upto + 1) - first_op(t + 1) + bodies(t, upto); }
constexpr int mn(int a, int b) { return a < b ? a : b; }
constexpr int FLAG_AT[4] = {0, 12, 22, 33};
constexpr int POLL_AT[4] = {0, 14, 24, 34};
constexpr int CHECK_AT[4] = {0, 14, 25, 35};
constexpr int wait_at(int i) {
  int w = 63;
  for (int n = 0; n < NST; ++n) if ((need_bar(n) == 0 ? -2 : need_bar(n) - 1) == i) w = mn(w, after_op(find(K_W, n, 1), i));
  if (i + 1 >= 0 && i + 1 < 9) w = mn(w, after_op(find(K_X, i + 1, 3), i));
  if (i == SJ0 - 1) w = mn(w, after_body(4, i));
  if (i == FLAG_AT[1] - 1) w = mn(w, after_body(10, i));
  if (i == FLAG_AT[2] - 1) w = mn(w, after_body(19, i));
  if (i == FLAG_AT[3] - 1) w = mn(w, after_body(31, i));
  for (int n = 0; n < 9; ++n) { if (i == IT_C + n - 1) w = mn(w, after_op(find(K_CH, n, 3), i));
                                if (i == IT_C + n - (clate(n) ? 1 : 2)) w = mn(w, after_op(find(K_CW, n, 4), i)); }
  return w;
}
constexpr bool sched_ok() {
  for (int n = NPRE; n < NST; ++n) { if (ISSUE[n] < free_item(n) || ISSUE[n] < 0) return false; if (late(n) && !BAR[item(n)]) return false; }
  for (int i = FIRST_IT; i < NIT; ++i) if (wait_at(i) < 0) return false;
  for (int n = 4; n < 9; ++n) if (CWI[n] < IT_C + n - 3 || CHI[n] < IT_C + n - 3) return false;
  for (int v = 1; v < 4; ++v) if (!BAR[CHECK_AT[v] + 1] || !BAR[FLAG_AT[v]]) return false;
  if (ISSUE[18] <= CHECK_AT[1] || WF_AT[0] <= CHECK_AT[1] || CHI[0] <= CHECK_AT[2] || CHI[8] <= CHECK_AT[3]) return false;
  return 23 % RING == 2 && CWI[0] >= free_item(30) && CWI[1] >= IT_C && CWI[3] > IT_C; }
static_assert(sched_ok(), "schedule");
constexpr unsigned region(int n) { return (n % RING) * SLOT >= 65536 ? 65536u : 0u; }
constexpr int nfrag(int n) { return n < 18 ? 16 : 12; }
}

__global__ __launch_bounds__(512, 2) void main_kernel(const u16* __restrict__ x, const u16* __restrict__ wqkv, const u16* __restrict__ wo, const u16* __restrict__ wg,
                                                      const u16* __restrict__ wu, const u16* __restrict__ wd, u16* __restrict__ out, char* __restrict__ ws) {
  using namespace pg;
  extern __shared__ __attribute__((aligned(16))) char shm[];
  const unsigned sb = (unsigned)(size_t)(LAS char*)shm;
  const int tid = threadIdx.x, lane = tid & 63, w = __builtin_amdgcn_readfirstlane(tid >> 6), b = blockIdx.x;
  STAMP_DECL; STAMP_RT(62); STAMP(0);
  const int xcd = b & 7, bi = b >> 3;
  const int r = (xcd >> 2) * 4 + (bi & 3), c = (xcd & 3) * 8 + (bi >> 2);
  const int ks = xcd & 3, rt = (xcd >> 2) * 8 + (bi >> 2), ct = bi & 3, wm = w & 3, kh = w >> 2;
  const int g = lane >> 4, q = (lane & 15) >> 2, p = lane & 3;
  const unsigned l16 = lane * 16;
  const f32x4 zero4 = {0.f, 0.f, 0.f, 0.f};

  u32x4 xv[2];
  { const char* xr0 = (const char*)x + (size_t)(b * 8 + w) * 1152; const unsigned o = lane < 36 ? l16 : 0u;
    gload16<0>(xv[0], xr0, o); gload16<576>(xv[1], xr0, o); }
  unsigned voff_big;
  { const int row = 8 * w + (lane >> 3), pc = lane & 7, f = ((row >> 1) & 1) | (((row >> 3) & 1) << 1);
    voff_big = row * 9216 + (((pc >> 1) ^ f) * 32) + (pc & 1) * 16; }
  const char* gbase = (const char*)wg + c * 256; const char* ubase = (const char*)wu + c * 256;
  unsigned ldw = sb + w * 1024;
  auto big_dma = [&](auto nn, auto hh) { constexpr int n = decltype(nn)::v, h = decltype(hh)::v;
    glds16((h ? ubase : gbase) + ((size_t)(n % 9) * 64 * 9216 + (n / 9) * 128), voff_big, ldw + ((n % RING) * SLOT + h * 8192)); };
  auto prefill = [&](auto aa, auto bb) { sfor<decltype(aa)::v, decltype(bb)::v>([&](auto nn) { big_dma(nn, IC<0>{}); big_dma(nn, IC<1>{}); }); };
  if (w >= 3) prefill(IC<0>{}, IC<2>{});
  if (w == 3) prefill(IC<2>{}, IC<NPRE>{});
  unsigned long long tag;
  { const char* ep = ws + WS_FLAG + FL_EPOCH; asm volatile("s_load_dwordx2 %0, %1, 0x0\n\ts_waitcnt lgkmcnt(0)" : "=s"(tag) : "s"(ep) : "memory"); }
  tag = tag * 6364136223846793005ull + 1442695040888963407ull;
  spin(tag);
  const unsigned tlo = (unsigned)tag, thi = (unsigned)(tag >> 32);
  const int tile = rt * 4 + ct;
  const char* xbase = ws + WS_XF + (size_t)(r * 16 + 2 * w) * 18432;
  const char* g2src = w < 6 ? ws + WS_W2 + (size_t)c * 110592 + w * 2048 : ws + WS_XF + (size_t)(r > 0 ? r * 16 - 1 : 0) * 18432;
  unsigned g2str = w < 6 ? 12288 : 2048, g2lds = sb + (w < 6 ? w : 6) * 2048;
  char* hbase = ws + WS_HF + ((size_t)((r * 8 + w) * 4 + (c >> 3)) * 36) * 2048 + (c & 7) * 4096;
  char* osb = ws + WS_OF + (size_t)(r * 8 + w) * 40960;
  char* inbox = ws + WS_PART + (size_t)(tile * 4 + ks) * 32768;
  char* pubb = ws + WS_PART + (size_t)(tile * 4 + wm) * 32768 + (((ks - wm - 1) & 3) * 9 + kh * 5) * 1024;
  const char* wdb = ws + WS_WD + (size_t)(ct * 4 + ks) * 36 * 9216;
  const char* hb = ws + WS_HF + ((size_t)((rt * 4 + wm) * 4 + ks) * 36 + 2 * kh) * 2048;
  const char* olb = ws + WS_OF + (size_t)(rt * 4 + ks) * 40960;
  const char* wob = ws + WS_WO + (size_t)(ct * 9 + w) * 20480; const char* wo8b = ws + WS_WO + (size_t)(ct * 9 + 8) * 20480;
  const char* xrb = (const char*)x + ((size_t)(rt * 128 + ks * 32) * 576 + ct * 144) * 2;
  char* outb = (char*)out + ((size_t)(rt * 128 + ks * 32) * 576 + ct * 144) * 2;
  char* flg = ws + WS_FLAG;
  spin(xbase); spin(g2src); spin(g2str); spin(g2lds); spin(hbase); spin(osb); spin(inbox); spin(pubb); spin(wdb); spin(hb); spin(olb); spin(wob); spin(wo8b);
  spin(xrb); spin(outb); spin(flg);
  SCHED();
  STAMP(49);
  if (w > 3) wait_vm<4>(); else if (w == 3) wait_vm<2 * NPRE>(); else wait_vm<0>();
  pin(xv[0]); pin(xv[1]);
  {
    const bool act = lane < 36;
    float s = 0.f;
    UNROLL for (int j = 0; j < 2; ++j) UNROLL for (int e = 0; e < 4; ++e) s = dot2(xv[j][e], 0x3f803f80u, s);
    const float mean = wave_sum(act ? s : 0.f) * (1.0f / 576.0f);
    float v = 0.f;
    UNROLL for (int j = 0; j < 2; ++j) UNROLL for (int e = 0; e < 4; ++e) { const float a = bf_lo(xv[j][e]) - mean, d = bf_hi(xv[j][e]) - mean; v += a * a + d * d; }
    const float mb = rbf(mean), vb = rbf(wave_sum(act ? v : 0.f) * (1.0f / 576.0f));
    const float rs = __builtin_amdgcn_rcpf(rbf(__builtin_amdgcn_sqrtf(rbf(vb + 1e-5f))));
    UNROLL for (int j = 0; j < 2; ++j) {
      u32x4 o;
      UNROLL for (int e = 0; e < 4; ++e) { const unsigned d = pk_bf16(bf_lo(xv[j][e]) - mb, bf_hi(xv[j][e]) - mb); o[e] = pk_bf16(bf_lo(d) * rs, bf_hi(d) * rs); }
      if (act) *(LAS u32x4*)(size_t)(sb + A_LN + (lane + 36 * j) * 128 + w * 16) = o;
    }
    asm volatile("s_waitcnt lgkmcnt(0)" ::: "memory");
  }
  barrier();
  STAMP(50);
  if (w < 3) {
    char* dst = ws + WS_XF + (size_t)(b >> 1) * 18432 + (b & 1) * 128 + w * 6144;
    const unsigned o = (lane >> 5) * 1024 + ((lane >> 3) & 3) * 256 + (lane & 7) * 16;
    u32x4 t[3];
    UNROLL for (int j = 0; j < 3; ++j) t[j] = *(LAS u32x4*)(size_t)(sb + A_LN + (w * 3 + j) * 1024 + l16);
    gstore16_sc1<0>(dst, o, t[0]); gstore16_sc1<2048>(dst, o, t[1]); gstore16_sc1<0>(dst + 4096, o, t[2]);
    prefill(IC<0>{}, IC<2>{});
    wait_vm<4>();
    if (lane == 0) __hip_atomic_store((GAS unsigned long long*)(ws + WS_FLAG + FL_A) + b * 4 + w, tag, __ATOMIC_RELAXED, __HIP_MEMORY_SCOPE_AGENT);
  }
  STAMP(51);
  const int us0 = w * 256 + b, us = us0 < 1764 ? us0 : 0, uq0 = (7 - w) * 256 + b, uq = uq0 < 576 ? uq0 : 0;
  const int ty = us < 1296 ? 0 : us < 1476 ? 1 : 2;
  const int su = ty == 0 ? us : ty == 1 ? us - 1296 : us - 1476;
  const int kap = ty < 2 ? su / 9 : (su % 144) >> 3, lc = ty < 2 ? su % 9 : 64 + (su & 7), mat = su / 144;
  const int hd = uq / 18, k5 = uq % 18;
  const char *sjsrc, *sqsrc; unsigned sjstep, voff_sj, voff_sq, qrd0, qrd1, sqlds = sb + A_Q + (w >= 5 ? w - 5 : 0) * 5120;
  { const unsigned stride = ty < 2 ? 1152 : 9216;
    const bool nat = ty == 2 || (ty == 1 && kap < 16);
    sjsrc = (const char*)(ty == 0 ? wd : ty == 1 ? wo : mat ? wu : wg) + lc * 128 +
            (size_t)(ty != 1 ? 32 * kap : kap < 16 ? 36 * kap : 144 * (kap - 16) + 16) * stride;
    sjstep = (nat ? 8 : ty == 0 ? 4 : 36) * stride;
    const int e = lane >> 3, pc = lane & 7;
    voff_sj = (nat ? e : 16 * (e >> 2) + (e & 3)) * stride + (((pc >> 1) ^ ((e >> 1) & 1)) * 32) + (pc & 1) * 16;
    sqsrc = (const char*)wqkv + (size_t)k5 * 32 * 3456 + ((36 * hd) & ~15);
    const int n = min(lane, 62), row = (n * 57) >> 9, ch = n - 9 * row;
    voff_sq = row * 3456 + ((ch * 11) >> 5) * (1152 - 48) + ch * 16;
    auto qrow = [&](int rho) { const int blk = rho < 28 ? (rho * 37) >> 8 : 4; return sqlds + blk * 1024 + (rho < 28 ? rho - 7 * blk : rho - 25) * 144 + ((36 * hd) & 8); };
    qrd0 = qrow(8 * g + q); qrd1 = qrow(8 * g + q + 4);
    spin(sjsrc); spin(sjstep); spin(sqsrc); spin(sqlds); opaque(voff_sj); opaque(voff_sq); opaque(qrd0); opaque(qrd1);
  }
  unsigned sjd[4], sqd;
  { const int k2 = kap < 128 ? kap >> 5 : (kap - 128) >> 2, step = kap < 128 ? 16 * ((kap >> 1) & 1) + 2 * ((kap >> 2) & 7) + (kap & 1) : 32 + ((kap - 128) & 3);
    UNROLL for (int t = 0; t < 4; ++t) { const int NT = 4 * lc + t;
      sjd[t] = ty == 0 ? (unsigned)WS_WD + (((NT / 9) * 4 + k2) * 36 + step) * 9216 + (NT % 9) * 1024
             : ty == 1 ? (unsigned)WS_WO + (NT * 20 + kap) * 1024 : (unsigned)WS_W2 + (((NT - 256) * 18 + kap) * 6 + 4 + mat) * 1024;
      spin(sjd[t]); }
    sqd = (unsigned)WS_W2 + (hd * 18 + k5) * 6144; spin(sqd); }
  unsigned son = (unsigned)((us0 - 1764) >> 31), qon = (unsigned)((uq0 - 576) >> 31); spin(son); spin(qon);
  int zk = ty == 1 ? (kap < 16 ? 1 : 2) : 0; spin(zk);
  SCHED();
  if (w != 3) prefill(IC<2>{}, IC<NPRE>{});
  STAMP(52);
  auto tags_ok = [&](const u32x4 v) -> bool { return __ballot(((v[0] ^ tlo) | (v[1] ^ thi) | (v[2] ^ tlo) | (v[3] ^ thi)) != 0u) == 0ull; };
  if (w == 3) {
    const int pw = max(32 * r - 2 + (int)min(lane, 33), 0);
    const char* fa = ws + WS_FLAG + FL_A;
    for (unsigned spins = 0; spins < 1000000u; ++spins) {
      u32x4 v0; u32x2 v1;
      asm volatile("global_load_dwordx4 %0, %2, %3 sc1\n\tglobal_load_dwordx2 %1, %2, %3 offset:16 sc1\n\ts_waitcnt vmcnt(0)" : "=&v"(v0), "=&v"(v1) : "v"(pw * 32), "s"(fa) : "memory");
      if (tags_ok(v0) && tags_ok((u32x4){v1[0], v1[1], v1[0], v1[1]})) break;
      __builtin_amdgcn_s_sleep(1);
    }
  }
  wait_vm<wait_at(-2)>();
  barrier();
  STAMP(53);

  bf16x8 xf[18][2];
  bf16x8 hf[4][4];
  bf16x8 wf[20];
  u32x2 xr[3];
  u32x4 pollv, pollw;
  const unsigned fo = (lane & 15) * 1152 + g * 8;
  auto ld_op = [&](auto kk) {
    constexpr Op o = P.op[decltype(kk)::v];
    if constexpr (o.kind == K_W) {
      constexpr int n = o.a, h = o.b;
      if constexpr (n < 18) big_dma(IC<n>{}, IC<h>{});
      else glds16<h * 1024, true>(g2src + (n - 18) * g2str, l16, g2lds + (n % RING) * SLOT);
    } else if constexpr (o.kind == K_X) {
      constexpr int k2 = 2 * o.a + (o.b & 1), m = o.b >> 1;
      gload16<(o.b & 1) * 1024, true>(xf[k2][m], xbase + (m * 18 + 2 * o.a) * 1024, l16);
    } else if constexpr (o.kind == K_SJ) {
      glds16(sjsrc + o.a * sjstep, (o.a & 1) ? voff_sj ^ 64u : voff_sj, ldw * 4 - 3 * sb + (A_S + o.a * 1024));
    } else if constexpr (o.kind == K_FILL) {
      gstore16_sc1<o.a * 1024>(inbox + w * 4096, l16, (u32x4){~0u, ~0u, ~0u, ~0u});
    } else if constexpr (o.kind == K_CW) {
      constexpr int n = o.a, j = o.b; constexpr unsigned so = (n % 4) * CSTG;
      if constexpr (j < 4) glds16<j * 1024, true>(wdb + ((size_t)n * CSTG + w * 4096), l16, sb + so + w * 4096);
      else glds16<0, true, true>(wdb + ((size_t)n * CSTG + 32768 + w * 512), l16, sb + so + 32768 + w * 512);
    } else if constexpr (o.kind == K_CH) {
      gload16<o.b * 1024, true>(hf[o.a % 4][o.b], hb + (size_t)o.a * 8192, l16);
    } else if constexpr (o.kind == K_CO) {
      if constexpr (o.a < 4) glds16<o.a * 1024, true>(olb + w * 4096, l16, sb + C_O + w * 4096);
      else glds16<0, true>(olb + 32768 + w * 1024, l16, sb + C_O + 32768 + w * 1024);
    } else if constexpr (o.kind == K_C8) {
      if constexpr (o.a < 2) glds16<o.a * 1024, true>(wo8b + w * 2048, l16, sb + C_W8 + w * 2048);
      else glds16<0, true, true>(wo8b + 16384 + w * 512, l16, sb + C_W8 + 16384 + w * 512);
    } else if constexpr (o.kind == K_WF) {
      gload16<(o.a & 3) * 1024, true>(wf[o.a], wob + (o.a & ~3) * 1024, l16);
    } else if constexpr (o.kind == K_XL) {
      gload8(xr[o.a], xrb + (o.a == 0 ? w * 32 : o.a == 1 ? w * 32 + 16 * 1152 : 256 + kh * 16 * 1152), fo);
    }
  };
  auto ld_all = [&](auto ii) { constexpr int I = decltype(ii)::v; sfor<first_op(I), first_op(I + 1)>([&](auto kk) { ld_op(kk); }); };
  auto ld_spread = [&](auto ii, auto ff, auto nf) {
    constexpr int I = decltype(ii)::v, fi = decltype(ff)::v, NF = decltype(nf)::v, NL = nld_at(I), F0 = first_op(I);
    sfor<0, NL>([&](auto kk) { if constexpr (decltype(kk)::v * (NF - 1) / (NL > 0 ? NL : 1) == fi) { ld_op(IC<F0 + decltype(kk)::v>{}); SCHED(); } });
  };
  auto poll_rq = [&](auto vv) {
    constexpr int V = decltype(vv)::v;
    const unsigned off = V == 1 ? l16 : (unsigned)(((lane & 16) ? ((xcd >> 2) * 4 + ct) : (rt >> 1)) * 256 + (lane & 15) * 16);
    const char* fp = flg + (V - 1) * 2048;
    gload16_rw<0>(pollv, fp, off);
    if constexpr (V == 1) gload16_rw<1024>(pollw, fp, off);
  };
  auto end_item = [&](auto ii) {
    constexpr int I = decltype(ii)::v, J = I + 1;
    if constexpr (wait_at(I) < 63) wait_vm<wait_at(I)>();
    if constexpr (J >= 0 && J < 9) { pin(xf[2 * J][0]); pin(xf[2 * J][1]); pin(xf[2 * J + 1][0]); pin(xf[2 * J + 1][1]); }
    if constexpr (J >= IT_C && J < IT_C + 9) { UNROLL for (int j = 0; j < 4; ++j) pin(hf[(J - IT_C) % 4][j]); }
    sfor<1, 4>([&](auto vv) { constexpr int V = decltype(vv)::v; if constexpr (I == CHECK_AT[V]) {
      if (w == 0) {
        wait_vm<after_body(POLL_AT[V] - 1, I) - body_at(POLL_AT[V])>();
        pin(pollv); if constexpr (V == 1) pin(pollw);
        unsigned spins = 0;
        while (!(tags_ok(pollv) && (V != 1 || tags_ok(pollw)))) {
          __builtin_amdgcn_s_sleep(4);
          poll_rq(vv);
          wait0_pin(pollv); if constexpr (V == 1) pin(pollw);
          if (++spins > 1000000u) break;
        }
        if constexpr (V == 1) {
          if (b == 0 && lane == 0) __hip_atomic_store((GAS unsigned long long*)(flg + FL_EPOCH), tag, __ATOMIC_RELAXED, __HIP_MEMORY_SCOPE_AGENT); }
      } } });
  };
  auto start_item = [&](auto ii) {
    constexpr int I = decltype(ii)::v;
    if constexpr (BAR[I] && I > 0) barrier();
    sfor<1, 4>([&](auto vv) { constexpr int V = decltype(vv)::v;
      if constexpr (I == FLAG_AT[V]) {
        if (tid == 64) __hip_atomic_store((GAS unsigned long long*)flg + (V - 1) * 256 + r * 32 + c, tag, __ATOMIC_RELAXED, __HIP_MEMORY_SCOPE_AGENT);
        SCHED(); }
      if constexpr (I == POLL_AT[V]) { pollv = (u32x4){0u, 0u, 0u, 0u}; pin(pollv); if constexpr (V == 1) { pollw = pollv; pin(pollw); } if (w == 0) poll_rq(vv); SCHED(); } });
    STAMP(I + 1);
  };

  unsigned tb[5];
  auto set_big = [&](unsigned sb) { const int f = (q >> 1) | ((g & 1) << 1);
    UNROLL for (int t = 0; t < 4; ++t) { tb[t] = sb + (8 * g + q) * 128 + ((t ^ f) * 32) + 8 * p; opaque(tb[t]); } };
  auto set_g2 = [&](unsigned sb) { UNROLL for (int t = 0; t < 4; ++t) { tb[t] = sb + l16 + (t ^ (w & 3)) * 1024; opaque(tb[t]); } tb[4] = sb + l16; opaque(tb[4]); };
  auto rebase = [&](auto aa, auto bb) { constexpr int n0 = decltype(aa)::v, n1 = decltype(bb)::v;
    if constexpr (region(n0) != region(n1)) { UNROLL for (int t = 0; t < (n1 < 18 ? 4 : 5); ++t) { tb[t] += region(n1) - region(n0); opaque(tb[t]); } } };
  auto baddr = [&](auto nn, auto ff, auto hh) -> unsigned {
    constexpr int n = decltype(nn)::v, fi = decltype(ff)::v, i = decltype(hh)::v; constexpr unsigned so = (n % RING) * SLOT - region(n);
    return tb[fi & 3] + (so + ((fi >> 2) & 1) * 8192 + (fi >> 3) * 4096 + i * 512); };
  auto g2rd = [&](auto nn, auto ff) -> bf16x8 {
    constexpr int n = decltype(nn)::v, fi = decltype(ff)::v; constexpr unsigned so = (n % RING) * SLOT - region(n);
    if constexpr (fi < 12 && fi % 6 < 4) return *(LAS bf16x8*)(size_t)(tb[fi % 6] + (so + (fi / 6) * 6144));
    else return *(LAS bf16x8*)(size_t)(tb[4] + (so + fi * 1024)); };

  f32x4 acc[8][2], acch, acy[9][2];
  bf16x8 af[4];
  bf16x8 hfr;
  auto first3 = [&](auto nn) {
    constexpr int n = decltype(nn)::v;
    SCHED();
    if constexpr (n >= 18) hfr = g2rd(nn, IC<12>{});
    sfor<0, 3>([&](auto ff) { if constexpr (n < 18) af[decltype(ff)::v] = cat(trd(baddr(nn, ff, IC<0>{})), trd(baddr(nn, ff, IC<1>{}))); else af[decltype(ff)::v] = g2rd(nn, ff); });
    SCHED();
  };
  u32x4 sjf;
  auto side_read = [&](auto tt, auto nn) {
    constexpr int t = decltype(tt)::v;
    if constexpr (t < 4) { const unsigned a = tb[t] + (ldw - sb) * 4 + (A_S - region(decltype(nn)::v)); sjf = __builtin_bit_cast(u32x4, cat(trd(a), trd(a + 512))); }
    else { const int G = min(4 * (t - 4) + p, 14); const unsigned a = ((G * 52) >> 8) * 8 + 8 * G; sjf = __builtin_bit_cast(u32x4, cat(trd(qrd0 + a), trd(qrd1 + a))); }
    SCHED();
  };
  auto side_store = [&](auto tt) {
    constexpr int t = decltype(tt)::v;
    if constexpr (t < 4) { sjf[0] = (zk == 1 && g == 2) ? 0u : sjf[0]; sjf[1] = zk == 2 ? 0u : sjf[1]; }
    char* dst = ws + (t < 4 ? sjd[t & 3] : sqd + (t & 3) * 1024);
    const unsigned on = t < 4 ? son : qon; const u32x4 v = sjf;
    asm volatile("s_mov_b32 exec_lo, %3\n\ts_mov_b32 exec_hi, %3\n\tglobal_store_dwordx4 %0, %1, %2 sc1\n\ts_mov_b64 exec, -1\n\ts_nop 0" ::"v"(l16), "v"(v), "s"(dst), "s"(on) : "memory");
    SCHED();
  };
  auto run_stage = [&](auto nn, auto ii) {
    constexpr int n = decltype(nn)::v, NF = nfrag(n);
    constexpr bool big = n < 18;
    constexpr int st = n % 9;
    constexpr bool last = st == 8;
    constexpr bool has_next = !last && !late(n + 1);
    sfor<0, NF>([&](auto ff) {
      constexpr int fi = decltype(ff)::v;
      constexpr int kstep = 2 * st + (big ? fi >> 3 : fi / 6), ai = big ? (fi & 7) : fi % 6;
      constexpr bool pf = (fi + 3 < NF) || has_next;
      constexpr int pn = fi + 3 < NF ? n : n + 1, pfi = fi + 3 < NF ? fi + 3 : fi + 3 - NF;
      const bf16x8 cur = af[fi & 3];
      mma<kstep == 0>(acc[ai][0], cur, xf[kstep][0]);
      SCHED();
      if constexpr (pf && pn != n && pfi == 0) rebase(nn, IC<pn>{});
      if constexpr (big) {
        s16x4 lo, hi;
        if constexpr (pf) lo = trd(baddr(IC<pn>{}, IC<pfi>{}, IC<0>{}));
        SCHED();
        mma<kstep == 0>(acc[ai][1], cur, xf[kstep][1]);
        SCHED();
        if constexpr (pf) { hi = trd(baddr(IC<pn>{}, IC<pfi>{}, IC<1>{})); af[(fi + 3) & 3] = cat(lo, hi); }
        SCHED();
      } else {
        if constexpr (pf) af[(fi + 3) & 3] = g2rd(IC<pn>{}, IC<pfi>{});
        SCHED();
        mma<kstep == 0>(acc[ai][1], cur, xf[kstep][1]);
        SCHED();
        if constexpr (ai == 0) { mma<kstep == 0>(acch, cur, hfr); SCHED(); }
        if constexpr (fi == 1) { hfr = g2rd(nn, IC<13>{}); SCHED(); }
        if constexpr (fi == 10 && has_next) { hfr = g2rd(IC<n + 1>{}, IC<12>{}); SCHED(); }
      }
      ld_spread(ii, ff, IC<NF>{});
      if constexpr (n < 5 && fi == 14) {
        const char* src = sqsrc + (n < 4 ? 7 * n : 25) * 3456; const unsigned dst = sqlds + n * 1024;
        asm volatile("s_mov_b32 m0, %2\n\ts_mov_b32 exec_lo, %3\n\ts_mov_b32 exec_hi, %3\n\tglobal_load_lds_dwordx4 %0, %1\n\ts_mov_b64 exec, -1" ::"v"(voff_sq), "s"(src), "s"(dst), "s"(qon) : "memory");
        SCHED(); }
      if constexpr (n >= SJ0 && n < SJ0 + 3 && fi < 15) {
        constexpr int u = (n - SJ0) * 3 + fi / 5;
        if constexpr (u < 8 && fi % 5 == 0) side_read(IC<(u + 4) & 7>{}, nn);
        if constexpr (u < 8 && fi % 5 == 4) side_store(IC<(u + 4) & 7>{});
      }
    });
    if constexpr (last) mma_drain();
    if constexpr (!last && !has_next) rebase(nn, IC<n + 1>{});
  };
  auto ep_unit = [&](auto gg, auto uu) {
    constexpr int gi = decltype(gg)::v, T = decltype(uu)::v >> 1, m = decltype(uu)::v & 1;
    gstore16_sc1<(T * 2 + m) * 1024>(hbase + gi * 32768, l16, silu_mul8(acc[2 * T][m], acc[4 + 2 * T][m], acc[2 * T + 1][m], acc[5 + 2 * T][m]));
  };

  ld_all(IC<-1>{});
  set_big(sb + region(0));
  first3(IC<0>{});
  end_item(IC<-1>{});
  sfor<0, IT_C>([&](auto ii) {
    constexpr int I = decltype(ii)::v; constexpr int n = stage_at(I);
    start_item(ii);
    if constexpr (n >= 0) {
      if constexpr (late(n) && n > 0) first3(IC<n>{});
      run_stage(IC<n>{}, ii);
    } else {
      ld_all(ii);
      if constexpr (I == 9) {
        sfor<0, 4>([&](auto uu) { ep_unit(IC<0>{}, uu); });
        rebase(IC<8>{}, IC<9>{}); first3(IC<9>{});
      } else if constexpr (I == 19) {
        sfor<0, 4>([&](auto uu) { ep_unit(IC<1>{}, uu); });
        set_g2(sb + region(18)); first3(IC<18>{});
      } else if constexpr (I == 29) {
        UNROLL for (int t = 0; t < 4; ++t) {
          const int G = 4 * (t ^ (w & 3)) + g;
          const unsigned a = sb + DUMP + (16 + 32 * w + (lane & 15)) * ROWB + (G < 15 ? ((G * 52) >> 8) * 8 + 8 * G : 88);
          UNROLL for (int m = 0; m < 2; ++m)
            *(LAS u32x2*)(size_t)(a + m * 16 * ROWB) = (u32x2){pk_bf16(acc[t][m][0], acc[t][m][1]), pk_bf16(acc[t][m][2], acc[t][m][3])};
          if (t == 0 && w < 4) *(LAS u32x2*)(size_t)(a - (16 + 32 * w) * ROWB) = (u32x2){pk_bf16(acch[0], acch[1]), pk_bf16(acch[2], acch[3])};
        }
        gstore16_sc1(hbase + (65536 - (c & 7) * 3072), l16, silu_mul8(acc[4][0], acc[5][0], acc[4][1], acc[5][1]));
        asm volatile("s_waitcnt lgkmcnt(0)" ::: "memory");
      }
    }
    end_item(ii);
  });

  bf16x8 kf[3]; u32x4 qf[2]; s16x4 vt[3][2]; f32x4 sa[2], sc[2]; float s0[2], s1[2], amx[2];
  auto att_seg = [&](auto ss) {
    constexpr int SEG = decltype(ss)::v;
    const int m = lane & 15;
    const bool hi = m & 1, b0 = 4 * g + (m & 1) <= m, b1 = 4 * g + 2 + (m & 1) <= m;
    if constexpr (SEG == 0) {
      const unsigned rowa = sb + DUMP + (32 * w + m) * ROWB + (g < 3 ? g : 0) * 16;
      const unsigned tra = sb + DUMP + (32 * w + 4 * g + q) * ROWB + 96 + 8 * p;
      UNROLL for (int t = 0; t < 3; ++t) kf[t] = *(LAS bf16x8*)(size_t)(rowa + 48 + t * 16 * ROWB);
      UNROLL for (int t = 0; t < 2; ++t) qf[t] = *(LAS u32x4*)(size_t)(rowa + (t + 1) * 16 * ROWB);
      UNROLL for (int t = 0; t < 3; ++t) UNROLL for (int d = 0; d < 2; ++d) vt[t][d] = trd(tra + t * 16 * ROWB + d * 32);
    } else if constexpr (SEG == 1) {
      const bool odd = c & 1;
      const unsigned m0 = (g == 3 || (odd && g == 0)) ? 0u : ~0u, m1 = (g == 3 || (!odd && g == 2)) ? 0u : ~0u, m2 = g >= 2 ? 0u : ~0u;
      UNROLL for (int t = 0; t < 2; ++t) {
        const u32x4 qm = {qf[t][0] & m0, qf[t][1] & m1, qf[t][2] & m2, qf[t][3] & m2};
        sa[t] = mfma(kf[t], __builtin_bit_cast(bf16x8, qm), zero4); sc[t] = mfma(kf[t + 1], __builtin_bit_cast(bf16x8, qm), zero4);
      }
    } else if constexpr (SEG == 3) {
      const float K = 0.23570226039551584f * 1.4426950408889634f;
      UNROLL for (int t = 0; t < 2; ++t) {
        float e0 = sa[t][0], e2 = sa[t][2], f0 = sc[t][0], f2 = sc[t][2]; opaque(e0); opaque(e2); opaque(f0); opaque(f2);
        const float a0 = hi ? sa[t][1] : e0, a1 = hi ? sa[t][3] : e2, c0 = hi ? sc[t][1] : f0, c1 = hi ? sc[t][3] : f2;
        s0[t] = (b0 ? c0 : a0) * K; s1[t] = (b1 ? c1 : a1) * K;
        if (t == 0 && r == 0 && w == 0) { s0[t] = b0 ? s0[t] : -1.4426950e9f; s1[t] = b1 ? s1[t] : -1.4426950e9f; }
        amx[t] = xor32_max(xor16_max(vmax(s0[t], s1[t])));
      }
    } else if constexpr (SEG == 4) {
      UNROLL for (int t = 0; t < 2; ++t) {
        s0[t] = __builtin_amdgcn_exp2f(s0[t] - amx[t]); s1[t] = __builtin_amdgcn_exp2f(s1[t] - amx[t]);
        amx[t] = __builtin_amdgcn_rcpf(xor32_add(xor16_add(s0[t] + s1[t])));
      }
    } else if constexpr (SEG == 5) {
      UNROLL for (int t = 0; t < 2; ++t) {
        const unsigned t0 = pk_bf16(s0[t] * amx[t], 0.f) << (hi ? 16 : 0), t1 = pk_bf16(s1[t] * amx[t], 0.f) << (hi ? 16 : 0);
        const u32x4 pf = {b0 ? 0u : t0, b1 ? 0u : t1, b0 ? t0 : 0u, b1 ? t1 : 0u};
        sa[t] = mfma(cat(vt[t][0], vt[t + 1][0]), __builtin_bit_cast(bf16x8, pf), zero4);
        sc[t] = mfma(cat(vt[t][1], vt[t + 1][1]), __builtin_bit_cast(bf16x8, pf), zero4);
      }
    } else if constexpr (SEG == 6) {
      const u32x4 o = pk8(sa[0], sa[1]);
      const auto r0 = __builtin_amdgcn_permlane16_swap(o[0], o[2], false, false), r1 = __builtin_amdgcn_permlane16_swap(o[1], o[3], false, false);
      gstore16_sc1(osb + c * 1024, l16, (u32x4){r0[0], r1[0], r0[1], r1[1]});
      if (g == 0) gstore16_sc1(osb + 32768 + c * 256, l16, pk8(sc[0], sc[1]));
    }
    SCHED();
  };

  unsigned ab0 = sb + kh * 18432 + l16, ab1 = ab0 + 2 * CSTG; opaque(ab0); opaque(ab1);
  auto ard = [&](auto nn, auto ff) -> bf16x8 { constexpr int n = decltype(nn)::v, fi = decltype(ff)::v; return *(LAS bf16x8*)(size_t)(((n % 4) >= 2 ? ab1 : ab0) + ((n % 2) * CSTG + fi * 1024)); };
  sfor<0, 9>([&](auto nn) {
    constexpr int n = decltype(nn)::v, I = IT_C + n;
    start_item(IC<I>{});
    if constexpr (clate(n)) { sfor<0, 3>([&](auto ff) { af[(n * 18 + decltype(ff)::v) & 3] = ard(nn, ff); }); SCHED(); }
    if constexpr (n == 8) {
      UNROLL for (int j = 0; j < 4; j += 2) {
        const bf16x8 a = hf[0][j], bb = hf[0][j + 1];
        hf[0][j] = __builtin_shufflevector(a, bb, 0, 1, 2, 3, 8, 9, 10, 11); hf[0][j + 1] = __builtin_shufflevector(a, bb, 4, 5, 6, 7, 12, 13, 14, 15);
        pin(hf[0][j]); pin(hf[0][j + 1]); }
      asm volatile("s_nop 1"); SCHED(); }
    sfor<0, 18>([&](auto ff) {
      constexpr int fi = decltype(ff)::v, gf = n * 18 + fi, sub = fi / 9, nt = fi % 9;
      constexpr bool pf = (fi + 3 < 18) || (n < 8 && !clate(n + 1));
      constexpr int pn = fi + 3 < 18 ? n : n + 1, pfi = fi + 3 < 18 ? fi + 3 : fi + 3 - 18;
      const bf16x8 cur = af[gf & 3];
      mma<(n == 0 && sub == 0)>(acy[nt][0], cur, hf[n % 4][2 * sub]);
      SCHED();
      if constexpr (pf) af[(gf + 3) & 3] = ard(IC<pn>{}, IC<pfi>{});
      SCHED();
      mma<(n == 0 && sub == 0)>(acy[nt][1], cur, hf[n % 4][2 * sub + 1]);
      SCHED();
      ld_spread(IC<I>{}, ff, IC<18>{});
      if constexpr (gf == 0) att_seg(IC<0>{});
      if constexpr (gf == 4) att_seg(IC<1>{});
      if constexpr (gf == 8) att_seg(IC<3>{});
      if constexpr (gf == 13) att_seg(IC<4>{});
      if constexpr (gf == 19) att_seg(IC<5>{});
      if constexpr (gf == 24) att_seg(IC<6>{});
    });
    end_item(IC<I>{});
  });
  mma_drain();
  STAMP(40);
  const unsigned xa = sb + C_X + l16;
  if (kh == 0) { UNROLL for (int j = 0; j < 4; ++j) *(LAS u32x4*)(size_t)(xa + w * 5120 + j * 1024) = pk8(acy[5 + j][0], acy[5 + j][1]); }
  else { UNROLL for (int j = 0; j < 5; ++j) *(LAS u32x4*)(size_t)(xa + w * 5120 + j * 1024) = pk8(acy[j][0], acy[j][1]);
         UNROLL for (int j = 0; j < 4; ++j) { acy[j][0] = acy[5 + j][0]; acy[j][1] = acy[5 + j][1]; } }
  asm volatile("s_waitcnt vmcnt(0) lgkmcnt(0)" ::: "memory");
  UNROLL for (int s = 0; s < 20; ++s) pin(wf[s]);
  UNROLL for (int j = 0; j < 3; ++j) pin(xr[j]);
  barrier();
  STAMP(41);
  UNROLL for (int j = 0; j < 5; ++j) { const u32x4 v = *(LAS u32x4*)(size_t)(xa + (w ^ 4) * 5120 + j * 1024); acy[j][0] += up_lo(v); acy[j][1] += up_hi(v); }
  if (wm != ks) {
    sfor<0, 4>([&](auto jj) { constexpr int j = decltype(jj)::v; gstore16_sc1<j * 1024>(pubb, l16, pk8(acy[j][0], acy[j][1])); });
    if (kh == 0) gstore16_sc1(pubb + 4096, l16, pk8(acy[4][0], acy[4][1]));
  } else {
    UNROLL for (int j = 0; j < 5; ++j) UNROLL for (int m = 0; m < 2; ++m)
      *(LAS f32x4*)(size_t)(sb + C_OWN + l16 + ((kh * 5 + j) * 2 + m) * 1024) = acy[j][m];
    asm volatile("s_waitcnt lgkmcnt(0)" ::: "memory");
  }
  STAMP(42);
  f32x4 sv0, sv1, sv2 = zero4;
  unsigned oa = sb + C_O + g * 512 + (lane & 15) * 16; opaque(oa);
  auto ord = [&](auto ss, unsigned ra, unsigned rb, int t) -> bf16x8 {
    constexpr int s = decltype(ss)::v;
    if constexpr (s < 16) return *(LAS bf16x8*)(size_t)(ra + s * 2048 + t * 256);
    else { const unsigned a = rb + 32768 + (s - 16) * 2048 + t * 8; return cat(*(LAS s16x4*)(size_t)a, *(LAS s16x4*)(size_t)(a + 256)); } };
  { bf16x8 of[4][2];
    sfor<0, 3>([&](auto ss) { of[decltype(ss)::v][0] = ord(ss, oa, oa, 0); SCHED(); of[decltype(ss)::v][1] = ord(ss, oa, oa, 1); SCHED(); });
    sfor<0, 20>([&](auto ss) { constexpr int s = decltype(ss)::v;
      mma<s == 0>(sv0, wf[s], of[s & 3][0]); SCHED();
      if constexpr (s + 3 < 20) of[(s + 3) & 3][0] = ord(IC<s + 3>{}, oa, oa, 0);
      SCHED();
      mma<s == 0>(sv1, wf[s], of[s & 3][1]); SCHED();
      if constexpr (s + 3 < 20) of[(s + 3) & 3][1] = ord(IC<s + 3>{}, oa, oa, 1);
      SCHED(); }); }
  if (wm == ks) sfor<0, 20>([&](auto ss) { sv2 = mfma(*(LAS bf16x8*)(size_t)(sb + l16 + (C_W8 + decltype(ss)::v * 1024)), ord(ss, oa + kh * 256, oa + kh * 8, 0), sv2); });
  mma_drain();
  STAMP(43);
  u32x4 pa[3], pb[3];
  auto fetch = [&]() {
    const char* s0 = inbox + w * 1024; const char* s1 = s0 + 9216; const char* s2 = s0 + 18432;
    const char* t0 = inbox + (wm == ks ? 8 : w) * 1024; const char* t1 = t0 + 9216; const char* t2 = t0 + 18432;
    int dk = wm ^ ks; spin(dk); const unsigned on = (unsigned)((dk - 1) >> 31);
    asm volatile("global_load_dwordx4 %0, %6, %7 sc1\n\tglobal_load_dwordx4 %1, %6, %8 sc1\n\tglobal_load_dwordx4 %2, %6, %9 sc1\n\t"
                 "s_mov_b32 exec_lo, %13\n\ts_mov_b32 exec_hi, %13\n\t"
                 "global_load_dwordx4 %3, %6, %10 sc1\n\tglobal_load_dwordx4 %4, %6, %11 sc1\n\tglobal_load_dwordx4 %5, %6, %12 sc1\n\t"
                 "s_mov_b64 exec, -1\n\ts_waitcnt vmcnt(0)"
                 : "=&v"(pa[0]), "=&v"(pa[1]), "=&v"(pa[2]), "+v"(pb[0]), "+v"(pb[1]), "+v"(pb[2])
                 : "v"(l16), "s"(s0), "s"(s1), "s"(s2), "s"(t0), "s"(t1), "s"(t2), "s"(on) : "memory"); };
  UNROLL for (int j = 0; j < 3; ++j) pb[j] = (u32x4){0u, 0u, 0u, 0u};
  for (unsigned spins = 0; spins < 200000u; ++spins) {
    fetch();
    unsigned mx = 0;
    UNROLL for (int j = 0; j < 3; ++j) {
      mx = max(max(mx, max(pa[j][0], pa[j][1])), max(pa[j][2], pa[j][3])); mx = max(max(mx, max(pb[j][0], pb[j][1])), max(pb[j][2], pb[j][3])); }
    if (__ballot(mx == ~0u) == 0ull) break;
    __builtin_amdgcn_s_sleep(2);
  }
  STAMP(44);
  barrier();
  STAMP(45);
  f32x4 y0 = *(LAS f32x4*)(size_t)(sb + C_OWN + l16 + w * 2048), y1 = *(LAS f32x4*)(size_t)(sb + C_OWN + l16 + w * 2048 + 1024);
  f32x4 y2 = *(LAS f32x4*)(size_t)(sb + C_OWN + l16 + 16384 + kh * 1024);
  UNROLL for (int j = 0; j < 3; ++j) { y0 += up_lo(pa[j]); y1 += up_hi(pa[j]); }
  auto fin = [&](f32x4 y, f32x4 s, u32x2 xv) -> u32x2 {
    u32x2 o = {pk_bf16(bf_lo(xv[0]) + silu_mul(y[0], s[0]), bf_hi(xv[0]) + silu_mul(y[1], s[1])),
               pk_bf16(bf_lo(xv[1]) + silu_mul(y[2], s[2]), bf_hi(xv[1]) + silu_mul(y[3], s[3]))};
    return o; };
  *(u32x2*)(outb + w * 32 + fo) = fin(y0, sv0, xr[0]);
  *(u32x2*)(outb + w * 32 + 16 * 1152 + fo) = fin(y1, sv1, xr[1]);
  if (wm == ks) {
    UNROLL for (int j = 0; j < 3; ++j) y2 += kh ? up_hi(pb[j]) : up_lo(pb[j]);
    *(u32x2*)(outb + 256 + kh * 16 * 1152 + fo) = fin(y2, sv2, xr[2]);
  }
  STAMP(46);
  STAMP(47); STAMP_RT(63); STAMP_OUT(0);
}

extern "C" void kernel_launch(void* const* d_in, const int* in_sizes, int n_in, void* d_out, int out_size,
                              void* d_ws, size_t ws_size, hipStream_t stream) {
  const u16* x = (const u16*)d_in[0]; const u16* wqkv = (const u16*)d_in[1]; const u16* wo = (const u16*)d_in[2];
  const u16* wg = (const u16*)d_in[4]; const u16* wu = (const u16*)d_in[5]; const u16* wd = (const u16*)d_in[6];
  (void)hipFuncSetAttribute((const void*)main_kernel, hipFuncAttributeMaxDynamicSharedMemorySize, pg::LDS_BYTES);
  main_kernel<<<256, 512, pg::LDS_BYTES, stream>>>(x, wqkv, wo, wg, wu, wd, (u16*)d_out, (char*)d_ws);
}
```

```cpp
#include <hip/hip_runtime.h>
#include <hip/hip_bf16.h>
#include <stdint.h>

typedef unsigned short u16;
typedef __attribute__((ext_vector_type(8))) __bf16 bf16x8;
typedef __attribute__((ext_vector_type(2))) __bf16 bf16x2;
typedef __attribute__((ext_vector_type(4))) short s16x4;
typedef __attribute__((ext_vector_type(4))) float f32x4;
typedef __attribute__((ext_vector_type(2))) float f32x2;
typedef __attribute__((ext_vector_type(4))) unsigned u32x4;
typedef __attribute__((ext_vector_type(2))) unsigned u32x2;
#define LAS __attribute__((address_space(3)))
#define GAS __attribute__((address_space(1)))
#define DI __device__ __forceinline__
#define UNROLL _Pragma("unroll")
#define SCHED() __builtin_amdgcn_sched_barrier(0)

template <int I> struct IC { static constexpr int v = I; };
template <int B, int E, class Fn> DI void sfor(Fn&& f) { if constexpr (B < E) { f(IC<B>{}); sfor<B + 1, E>(f); } }

constexpr int S_ = 2048, D_ = 576, F_ = 4608;
constexpr size_t WS_XF = 0;
constexpr size_t WS_HF = WS_XF + (size_t)128 * 18 * 1024;
constexpr size_t WS_OF = WS_HF + (size_t)64 * 144 * 2048;
constexpr size_t WS_WD = WS_OF + (size_t)128 * 20 * 1024;
constexpr size_t WS_WO = WS_WD + (size_t)4 * 144 * 9 * 1024;
constexpr size_t WS_W2 = WS_WO + (size_t)36 * 20 * 1024;
constexpr size_t WS_PART = WS_W2 + (size_t)32 * 108 * 1024;
constexpr size_t WS_FLAG = WS_PART + (size_t)256 * 32768;
constexpr size_t FL_A = 6144, FL_EPOCH = FL_A + 8192;

DI float bf_lo(unsigned u) { return __uint_as_float(u << 16); }
DI float bf_hi(unsigned u) { return __uint_as_float(u & 0xffff0000u); }
DI unsigned pk_bf16(float a, float b) { f32x2 v = {a, b}; bf16x2 r = __builtin_convertvector(v, bf16x2); return __builtin_bit_cast(unsigned, r); }
DI float rbf(float a) { return bf_lo(pk_bf16(a, a)); }
DI float dot2(unsigned a, unsigned b, float c) { return __builtin_amdgcn_fdot2_f32_bf16(__builtin_bit_cast(bf16x2, a), __builtin_bit_cast(bf16x2, b), c, false); }
DI f32x4 mfma(bf16x8 a, bf16x8 b, f32x4 c) { return __builtin_amdgcn_mfma_f32_16x16x32_bf16(a, b, c, 0, 0, 0); }
template <bool FIRST> DI void mma(f32x4& c, const bf16x8& a, const bf16x8& b) {
  if constexpr (FIRST) asm volatile("v_mfma_f32_16x16x32_bf16 %0, %1, %2, 0" : "=&v"(c) : "v"(a), "v"(b));
  else asm volatile("v_mfma_f32_16x16x32_bf16 %0, %1, %2, %0" : "+v"(c) : "v"(a), "v"(b));
}
DI void mma_drain() { SCHED(); asm volatile("s_nop 7\n\ts_nop 1"); SCHED(); }
DI s16x4 trd(unsigned addr) { return __builtin_amdgcn_ds_read_tr16_b64_v4i16((LAS s16x4*)(size_t)addr); }
DI bf16x8 cat(s16x4 lo, s16x4 hi) { return __builtin_bit_cast(bf16x8, __builtin_shufflevector(lo, hi, 0, 1, 2, 3, 4, 5, 6, 7)); }
template <int OFF = 0, bool SC1 = false, bool HALF = false> DI void glds16(const char* base, unsigned voff, unsigned lds) {
  if constexpr (HALF) asm volatile("s_mov_b32 m0, %2\n\ts_mov_b32 exec_hi, 0\n\tglobal_load_lds_dwordx4 %0, %1 offset:%c3 sc1\n\ts_mov_b32 exec_hi, -1" ::"v"(voff), "s"(base), "s"(lds), "n"(OFF) : "memory");
  else if constexpr (SC1) asm volatile("s_mov_b32 m0, %2\n\ts_nop 0\n\tglobal_load_lds_dwordx4 %0, %1 offset:%c3 sc1" ::"v"(voff), "s"(base), "s"(lds), "n"(OFF) : "memory");
  else asm volatile("s_mov_b32 m0, %2\n\ts_nop 0\n\tglobal_load_lds_dwordx4 %0, %1 offset:%c3" ::"v"(voff), "s"(base), "s"(lds), "n"(OFF) : "memory");
}
template <int OFF = 0> DI void gstore16_sc1(char* base, unsigned voff, u32x4 v) { asm volatile("global_store_dwordx4 %0, %1, %2 offset:%c3 sc1\n\ts_nop 1" ::"v"(voff), "v"(v), "s"(base), "n"(OFF) : "memory"); }
DI void opaque(unsigned& v) { asm volatile("" : "+v"(v)); }
DI void opaque(float& v) { asm volatile("" : "+v"(v)); }
template <class T> DI void spin(T& v) { asm volatile("" : "+s"(v)); }
template <int N> DI void wait_vm() { asm volatile("s_waitcnt vmcnt(%0)" ::"n"(N > 63 ? 63 : N) : "memory"); }
DI void barrier() { SCHED(); asm volatile("" ::: "memory"); __builtin_amdgcn_s_barrier(); asm volatile("" ::: "memory"); SCHED(); }
template <int OFF = 0, bool SC1 = false, class T> DI void gload16(T& dst, const char* base, unsigned voff) {
  if constexpr (SC1) asm volatile("global_load_dwordx4 %0, %1, %2 offset:%c3 sc1" : "=v"(dst) : "v"(voff), "s"(base), "n"(OFF) : "memory");
  else asm volatile("global_load_dwordx4 %0, %1, %2 offset:%c3" : "=v"(dst) : "v"(voff), "s"(base), "n"(OFF) : "memory");
}
template <int OFF> DI void gload16_rw(u32x4& dst, const char* base, unsigned voff) {
  asm volatile("global_load_dwordx4 %0, %1, %2 offset:%c3 sc1" : "+v"(dst) : "v"(voff), "s"(base), "n"(OFF) : "memory");
}
DI void wait0_pin(u32x4& v) { asm volatile("s_waitcnt vmcnt(0)" : "+v"(v)::"memory"); }
DI void gload8(u32x2& dst, const char* base, unsigned voff) { asm volatile("global_load_dwordx2 %0, %1, %2" : "=v"(dst) : "v"(voff), "s"(base) : "memory"); }
DI void gtouch(unsigned& dst, const char* base, unsigned voff) { asm volatile("global_load_dword %0, %1, %2" : "+v"(dst) : "v"(voff), "s"(base) : "memory"); }
DI void pin(u32x2& v) { asm volatile("" : "+v"(v)); }
DI void pin(u32x4& v) { asm volatile("" : "+v"(v)); }
DI void pin(bf16x8& v) { asm volatile("" : "+v"(v)); }
#define STAMP_DECL
#define STAMP(i)
#define STAMP_RT(i)
#define STAMP_OUT(k)
DI float silu_mul(float g, float u) {
  float e = __builtin_amdgcn_exp2f(g * -1.4426950408889634f);
  return g * u * __builtin_amdgcn_rcpf(1.0f + e);
}

DI u32x4 silu_mul8(const f32x4 ga, const f32x4 ua, const f32x4 gb, const f32x4 ub) {
  float e0, e1, e2, e3, e4, e5, e6, e7;
  asm("v_mul_f32 %0, 0xbfb8aa3b, %8\n\tv_mul_f32 %1, 0xbfb8aa3b, %9\n\tv_mul_f32 %2, 0xbfb8aa3b, %10\n\tv_mul_f32 %3, 0xbfb8aa3b, %11\n\t"
      "v_mul_f32 %4, 0xbfb8aa3b, %12\n\tv_mul_f32 %5, 0xbfb8aa3b, %13\n\tv_mul_f32 %6, 0xbfb8aa3b, %14\n\tv_mul_f32 %7, 0xbfb8aa3b, %15\n\t"
      "v_exp_f32 %0, %0\n\tv_exp_f32 %1, %1\n\tv_exp_f32 %2, %2\n\tv_exp_f32 %3, %3\n\tv_exp_f32 %4, %4\n\tv_exp_f32 %5, %5\n\tv_exp_f32 %6, %6\n\tv_exp_f32 %7, %7\n\t"
      "v_add_f32 %0, 1.0, %0\n\tv_add_f32 %1, 1.0, %1\n\tv_add_f32 %2, 1.0, %2\n\tv_add_f32 %3, 1.0, %3\n\t"
      "v_add_f32 %4, 1.0, %4\n\tv_add_f32 %5, 1.0, %5\n\tv_add_f32 %6, 1.0, %6\n\tv_add_f32 %7, 1.0, %7\n\t"
      "v_rcp_f32 %0, %0\n\tv_rcp_f32 %1, %1\n\tv_rcp_f32 %2, %2\n\tv_rcp_f32 %3, %3\n\tv_rcp_f32 %4, %4\n\tv_rcp_f32 %5, %5\n\tv_rcp_f32 %6, %6\n\tv_rcp_f32 %7, %7\n\t"
      "v_mul_f32 %0, %8, %0\n\tv_mul_f32 %1, %9, %1\n\tv_mul_f32 %2, %10, %2\n\tv_mul_f32 %3, %11, %3\n\t"
      "v_mul_f32 %4, %12, %4\n\tv_mul_f32 %5, %13, %5\n\tv_mul_f32 %6, %14, %6\n\tv_mul_f32 %7, %15, %7\n\t"
      "v_mul_f32 %0, %16, %0\n\tv_mul_f32 %1, %17, %1\n\tv_mul_f32 %2, %18, %2\n\tv_mul_f32 %3, %19, %3\n\t"
      "v_mul_f32 %4, %20, %4\n\tv_mul_f32 %5, %21, %5\n\tv_mul_f32 %6, %22, %6\n\tv_mul_f32 %7, %23, %7"
      : "=&v"(e0), "=&v"(e1), "=&v"(e2), "=&v"(e3), "=&v"(e4), "=&v"(e5), "=&v"(e6), "=&v"(e7)
      : "v"(ga[0]), "v"(ga[1]), "v"(ga[2]), "v"(ga[3]), "v"(gb[0]), "v"(gb[1]), "v"(gb[2]), "v"(gb[3]),
        "v"(ua[0]), "v"(ua[1]), "v"(ua[2]), "v"(ua[3]), "v"(ub[0]), "v"(ub[1]), "v"(ub[2]), "v"(ub[3]));
  return (u32x4){pk_bf16(e0, e1), pk_bf16(e2, e3), pk_bf16(e4, e5), pk_bf16(e6, e7)};
}
DI u32x4 pk8(const f32x4 a, const f32x4 b) { return (u32x4){pk_bf16(a[0], a[1]), pk_bf16(a[2], a[3]), pk_bf16(b[0], b[1]), pk_bf16(b[2], b[3])}; }
DI f32x4 up_lo(const u32x4 v) { return (f32x4){bf_lo(v[0]), bf_hi(v[0]), bf_lo(v[1]), bf_hi(v[1])}; }
DI f32x4 up_hi(const u32x4 v) { return (f32x4){bf_lo(v[2]), bf_hi(v[2]), bf_lo(v[3]), bf_hi(v[3])}; }
template <int CTRL> DI float dpp_add(float v) { return v + __builtin_bit_cast(float, __builtin_amdgcn_update_dpp(0, __builtin_bit_cast(int, v), CTRL, 0xf, 0xf, true)); }
DI float vmax(float a, float b) { float r; asm("v_max_f32 %0, %1, %2" : "=v"(r) : "v"(a), "v"(b)); return r; }
DI float xor32_max(float v) { auto r = __builtin_amdgcn_permlane32_swap(__float_as_uint(v), __float_as_uint(v), false, false); return vmax(__uint_as_float(r[0]), __uint_as_float(r[1])); }
DI float xor16_max(float v) { auto r = __builtin_amdgcn_permlane16_swap(__float_as_uint(v), __float_as_uint(v), false, false); return vmax(__uint_as_float(r[0]), __uint_as_float(r[1])); }
DI float xor32_add(float v) { auto r = __builtin_amdgcn_permlane32_swap(__float_as_uint(v), __float_as_uint(v), false, false); return __uint_as_float(r[0]) + __uint_as_float(r[1]); }
DI float xor16_add(float v) { auto r = __builtin_amdgcn_permlane16_swap(__float_as_uint(v), __float_as_uint(v), false, false); return __uint_as_float(r[0]) + __uint_as_float(r[1]); }

DI float wave_sum(float v) {
  v = dpp_add<0xB1>(v); v = dpp_add<0x4E>(v); v = dpp_add<0x141>(v); v = dpp_add<0x140>(v);
  return xor32_add(xor16_add(v));
}

namespace pg {
constexpr int RING = 7, SLOT = 16384, LDS_BYTES = 163840;
constexpr int NST = 27;
constexpr int ROWB = 144;
constexpr int DUMP = LDS_BYTES - 272 * ROWB;
constexpr unsigned soff(int n) { return n < 21 ? (n % RING) * SLOT : n == 21 ? 131072 : n == 22 ? 147456 : n == 23 ? 49152 : n == 24 ? 73728 : n == 25 ? 88064 : 110336; }
static_assert(soff(26) + 14336 <= DUMP, "dump");
constexpr int A_LN = 6 * SLOT, A_S = RING * SLOT, A_Q = A_S + 32768;
static_assert(A_Q + 15360 <= LDS_BYTES, "scratch");
constexpr int CSTG = 36864;
constexpr int C_O = CSTG, C_W8 = C_O + 40960, C_X = 3 * CSTG, C_OWN = C_X + 8 * 5120;
static_assert(C_W8 + 20480 <= C_X && C_OWN + 8192 <= LDS_BYTES, "tail areas");
constexpr int FIRST_IT = -2, IT_C = 30, NIT = 39, NPRE = 4;
constexpr int item(int n) { return n < 9 ? n : n < 18 ? n + 1 : n + 2; }
constexpr int stage_at(int i) { return i < 0 ? -1 : i < 9 ? i : i == 9 ? -1 : i < 19 ? i - 1 : i == 19 ? -1 : i < 29 ? i - 2 : -1; }
constexpr int BAR[NIT] =          {1, 1, 1, 1, 1, 1, 1, 1, 0, 0, 0, 0, 1, 1, 1, 1, 1, 1, 0, 0, 0, 0, 1, 0, 1, 0, 1, 0, 0, 0, 1, 1, 1, 1, 1, 1, 1, 1, 1};
constexpr int ISSUE[NST] =        {-2, -2, -2, -2, 0, 1, 2, 3, 4, 4, 5, 6, 7, 8, 12, 12, 13, 13, 15, 15, 16, 17, 18, 22, 22, 24, 24};
constexpr bool late(int n) { return n == 0 || n == 11 || n == 20; }
constexpr int free_item(int n) { if (n < RING) return -100; if (n > 20) return n < 23 ? 12 : n < 25 ? 22 : 24; int i = item(n - RING) + 1; while (!BAR[i]) ++i; return i; }
constexpr int need_bar(int n) { int b = late(n) ? item(n) : item(n) - 1; while (!BAR[b]) --b; return b; }
constexpr int CWI[9] = {23, 26, 30, 31, 32, 33, 34, 35, 36};
constexpr int CHI[9] = {27, 28, 30, 31, 32, 33, 34, 35, 36};
constexpr int WF_AT[20] = {20, 20, 21, 21, 22, 22, 24, 24, 25, 25, 25, 25, 27, 28, 38, 38, 38, 38, 38, 38};
constexpr bool clate(int n) { return n <= 8; }
constexpr int FILL_AT[4] = {14, 14, 16, 17};
constexpr int SJ0 = 7;
constexpr int X_AT[9] = {-1, -1, 0, 0, 1, 2, 3, 4, 5};
constexpr int SJ_AT[4] = {1, 2, 3, 3};
enum { K_SJ, K_W, K_X, K_FILL, K_CW, K_CH, K_CO, K_C8, K_WF, K_XL };
struct Op { int item, kind, a, b; };
struct Prog { Op op[256]; int n; };
constexpr Prog build() {
  Prog p{}; p.n = 0;
  for (int i = FIRST_IT; i < NIT; ++i) {
    for (int j = 0; j < 9; ++j) if (X_AT[j] == i) for (int e = 0; e < 4; ++e) p.op[p.n++] = Op{i, K_X, j, e};
    for (int n = 0; n < NST; ++n) if (ISSUE[n] == i) for (int h = 0; h < 2; ++h) p.op[p.n++] = Op{i, K_W, n, h};
    for (int e = 0; e < 4; ++e) if (SJ_AT[e] == i) p.op[p.n++] = Op{i, K_SJ, e, 0};
    for (int e = 0; e < 4; ++e) if (FILL_AT[e] == i) p.op[p.n++] = Op{i, K_FILL, e, 0};
    for (int n = 0; n < 9; ++n) { if (CWI[n] == i) for (int j = 0; j < 5; ++j) p.op[p.n++] = Op{i, K_CW, n, j};
                                  if (CHI[n] == i) for (int j = 0; j < 4; ++j) p.op[p.n++] = Op{i, K_CH, n, j}; }
    if (i == 37) { for (int j = 0; j < 5; ++j) p.op[p.n++] = Op{i, K_CO, j, 0}; for (int j = 0; j < 3; ++j) p.op[p.n++] = Op{i, K_C8, j, 0}; }
    for (int s = 0; s < 20; ++s) if (WF_AT[s] == i) p.op[p.n++] = Op{i, K_WF, s, 0};
    if (i == 38) for (int j = 0; j < 3; ++j) p.op[p.n++] = Op{i, K_XL, j, 0};
  }
  return p;
}
constexpr Prog P = build();
constexpr int body_at(int i) { return i == 9 ? 4 : i == 19 ? 4 : i == 29 ? 1 : i == 31 ? 2 : 0; }
constexpr int first_op(int i) { int k = 0; while (k < P.n && P.op[k].item < i) ++k; return k; }
constexpr int nld_at(int i) { return first_op(i + 1) - first_op(i); }
constexpr int find(int kind, int a, int b) { for (int k = 0; k < P.n; ++k) if (P.op[k].kind == kind && P.op[k].a == a && P.op[k].b == b) return k; return -1; }
constexpr int bodies(int from, int upto) { int c = 0; for (int q = from + 1; q <= upto; ++q) c += body_at(q); return c; }
constexpr int after_op(int k, int upto) { return k < 0 || P.op[k].item > upto ? -1000 : first_op(upto + 1) - k - 1 + bodies(P.op[k].item, upto); }
constexpr int after_body(int t, int upto) { return first_op(upto + 1) - first_op(t + 1) + bodies(t, upto); }
constexpr int mn(int a, int b) { return a < b ? a : b; }
constexpr int FLAG_AT[4] = {0, 12, 22, 33};
constexpr int POLL_AT[4] = {0, 14, 24, 34};
constexpr int CHECK_AT[4] = {0, 14, 25, 35};
constexpr int wait_at(int i) {
  int w = 63;
  for (int n = 0; n < NST; ++n) if ((need_bar(n) == 0 ? -2 : need_bar(n) - 1) == i) w = mn(w, after_op(find(K_W, n, 1), i));
  if (i + 1 >= 0 && i + 1 < 9) w = mn(w, after_op(find(K_X, i + 1, 3), i));
  if (i == SJ0 - 1) w = mn(w, after_body(4, i));
  if (i == FLAG_AT[1] - 1) w = mn(w, after_body(10, i));
  if (i == FLAG_AT[2] - 1) w = mn(w, after_body(19, i));
  if (i == FLAG_AT[3] - 1) w = mn(w, after_body(31, i));
  for (int n = 0; n < 9; ++n) { if (i == IT_C + n - 1) w = mn(w, after_op(find(K_CH, n, 3), i));
                                if (i == IT_C + n - (clate(n) ? 1 : 2)) w = mn(w, after_op(find(K_CW, n, 4), i)); }
  if (i == NIT - 1) w = mn(w, after_op(find(K_C8, 2, 0), i));
  return w;
}
constexpr bool sched_ok() {
  for (int n = NPRE; n < NST; ++n) { if (ISSUE[n] < free_item(n) || ISSUE[n] < 0) return false; if (late(n) && !BAR[item(n)]) return false; }
  for (int i = FIRST_IT; i < NIT; ++i) if (wait_at(i) < 0) return false;
  for (int n = 4; n < 9; ++n) if (CWI[n] < IT_C + n - 3 || CHI[n] < IT_C + n - 3) return false;
  for (int v = 1; v < 4; ++v) if (!BAR[CHECK_AT[v] + 1] || !BAR[FLAG_AT[v]]) return false;
  if (ISSUE[18] <= CHECK_AT[1] || WF_AT[0] <= CHECK_AT[1] || CHI[0] <= CHECK_AT[2] || CHI[8] <= CHECK_AT[3]) return false;
  return BAR[12] && BAR[22] && BAR[24] && BAR[26] && CWI[0] >= 22 && CWI[1] >= 26 && CWI[2] >= IT_C && CWI[3] > IT_C; }
static_assert(sched_ok(), "schedule");
constexpr unsigned region(int n) { return soff(n) & ~0xffffu; }
constexpr int nfrag(int n) { return n < 18 ? 16 : 12; }
}

__global__ __launch_bounds__(512, 2) void main_kernel(const u16* __restrict__ x, const u16* __restrict__ wqkv, const u16* __restrict__ wo, const u16* __restrict__ wg,
                                                      const u16* __restrict__ wu, const u16* __restrict__ wd, u16* __restrict__ out, char* __restrict__ ws) {
  using namespace pg;
  extern __shared__ __attribute__((aligned(16))) char shm[];
  const unsigned sb = (unsigned)(size_t)(LAS char*)shm;
  const int tid = threadIdx.x, lane = tid & 63, w = __builtin_amdgcn_readfirstlane(tid >> 6), b = blockIdx.x;
  STAMP_DECL; STAMP_RT(62); STAMP(0);
  const int xcd = b & 7, bi = b >> 3;
  const int r = (xcd >> 2) * 4 + (bi & 3), c = (xcd & 3) * 8 + (bi >> 2);
  const int ks = xcd & 3, rt = (xcd >> 2) * 8 + (bi >> 2), ct = bi & 3, wm = w & 3, kh = w >> 2;
  const int g = lane >> 4, q = (lane & 15) >> 2, p = lane & 3;
  const unsigned l16 = lane * 16;
  const f32x4 zero4 = {0.f, 0.f, 0.f, 0.f};

  u32x4 xv[2];
  { const char* xr0 = (const char*)x + (size_t)(b * 8 + w) * 1152; const unsigned o = lane < 36 ? l16 : 0u;
    gload16<0>(xv[0], xr0, o); gload16<576>(xv[1], xr0, o); }
  unsigned voff_big;
  { const int row = 8 * w + (lane >> 3), pc = lane & 7, f = ((row >> 1) & 1) | (((row >> 3) & 1) << 1);
    voff_big = row * 9216 + (((pc >> 1) ^ f) * 32) + (pc & 1) * 16; }
  const char* gbase = (const char*)wg + c * 256; const char* ubase = (const char*)wu + c * 256;
  unsigned ldw = sb + w * 1024;
  auto big_dma = [&](auto nn, auto hh) { constexpr int n = decltype(nn)::v, h = decltype(hh)::v;
    glds16((h ? ubase : gbase) + ((size_t)(n % 9) * 64 * 9216 + (n / 9) * 128), voff_big, ldw + ((n % RING) * SLOT + h * 8192)); };
  auto prefill = [&](auto aa, auto bb) { sfor<decltype(aa)::v, decltype(bb)::v>([&](auto nn) { big_dma(nn, IC<0>{}); big_dma(nn, IC<1>{}); }); };
  if (w >= 3) prefill(IC<0>{}, IC<2>{});
  if (w == 3) prefill(IC<2>{}, IC<NPRE>{});
  unsigned long long tag;
  { const char* ep = ws + WS_FLAG + FL_EPOCH; asm volatile("s_load_dwordx2 %0, %1, 0x0\n\ts_waitcnt lgkmcnt(0)" : "=s"(tag) : "s"(ep) : "memory"); }
  tag = tag * 6364136223846793005ull + 1442695040888963407ull;
  spin(tag);
  const unsigned tlo = (unsigned)tag, thi = (unsigned)(tag >> 32);
  const int tile = rt * 4 + ct;
  const char* xbase = ws + WS_XF + (size_t)(r * 16 + 2 * w) * 18432;
  const char* g2src = w < 6 ? ws + WS_W2 + (size_t)c * 110592 + w * 2048 : ws + WS_XF + (size_t)(r > 0 ? r * 16 - 1 : 0) * 18432;
  unsigned g2str = w < 6 ? 12288 : 2048, g2lds = sb + (w < 6 ? w : 6) * 2048;
  char* hbase = ws + WS_HF + ((size_t)((r * 8 + w) * 4 + (c >> 3)) * 36) * 2048 + (c & 7) * 4096;
  char* osb = ws + WS_OF + (size_t)(r * 8 + w) * 40960;
  char* inbox = ws + WS_PART + (size_t)(tile * 4 + ks) * 32768;
  char* pubb = ws + WS_PART + (size_t)(tile * 4 + wm) * 32768 + (((ks - wm - 1) & 3) * 9 + kh * 5) * 1024;
  const char* wdb = ws + WS_WD + (size_t)(ct * 4 + ks) * 36 * 9216;
  const char* hb = ws + WS_HF + ((size_t)((rt * 4 + wm) * 4 + ks) * 36 + 2 * kh) * 2048;
  const char* olb = ws + WS_OF + (size_t)(rt * 4 + ks) * 40960;
  const char* wob = ws + WS_WO + (size_t)(ct * 9 + w) * 20480; const char* wo8b = ws + WS_WO + (size_t)(ct * 9 + 8) * 20480;
  const char* xrb = (const char*)x + ((size_t)(rt * 128 + ks * 32) * 576 + ct * 144) * 2;
  char* outb = (char*)out + ((size_t)(rt * 128 + ks * 32) * 576 + ct * 144) * 2;
  char* flg = ws + WS_FLAG;
  spin(xbase); spin(g2src); spin(g2str); spin(g2lds); spin(hbase); spin(osb); spin(inbox); spin(pubb); spin(wdb); spin(hb); spin(olb); spin(wob); spin(wo8b);
  spin(xrb); spin(outb); spin(flg);
  SCHED();
  STAMP(49);
  if (w > 3) wait_vm<4>(); else if (w == 3) wait_vm<2 * NPRE>(); else wait_vm<0>();
  pin(xv[0]); pin(xv[1]);
  {
    const bool act = lane < 36;
    float s = 0.f;
    UNROLL for (int j = 0; j < 2; ++j) UNROLL for (int e = 0; e < 4; ++e) s = dot2(xv[j][e], 0x3f803f80u, s);
    const float mean = wave_sum(act ? s : 0.f) * (1.0f / 576.0f);
    float v = 0.f;
    UNROLL for (int j = 0; j < 2; ++j) UNROLL for (int e = 0; e < 4; ++e) { const float a = bf_lo(xv[j][e]) - mean, d = bf_hi(xv[j][e]) - mean; v += a * a + d * d; }
    const float mb = rbf(mean), vb = rbf(wave_sum(act ? v : 0.f) * (1.0f / 576.0f));
    const float rs = __builtin_amdgcn_rcpf(rbf(__builtin_amdgcn_sqrtf(rbf(vb + 1e-5f))));
    UNROLL for (int j = 0; j < 2; ++j) {
      u32x4 o;
      UNROLL for (int e = 0; e < 4; ++e) { const unsigned d = pk_bf16(bf_lo(xv[j][e]) - mb, bf_hi(xv[j][e]) - mb); o[e] = pk_bf16(bf_lo(d) * rs, bf_hi(d) * rs); }
      if (act) *(LAS u32x4*)(size_t)(sb + A_LN + (lane + 36 * j) * 128 + w * 16) = o;
    }
    asm volatile("s_waitcnt lgkmcnt(0)" ::: "memory");
  }
  barrier();
  STAMP(50);
  if (w < 3) {
    char* dst = ws + WS_XF + (size_t)(b >> 1) * 18432 + (b & 1) * 128 + w * 6144;
    const unsigned o = (lane >> 5) * 1024 + ((lane >> 3) & 3) * 256 + (lane & 7) * 16;
    u32x4 t[3];
    UNROLL for (int j = 0; j < 3; ++j) t[j] = *(LAS u32x4*)(size_t)(sb + A_LN + (w * 3 + j) * 1024 + l16);
    gstore16_sc1<0>(dst, o, t[0]); gstore16_sc1<2048>(dst, o, t[1]); gstore16_sc1<0>(dst + 4096, o, t[2]);
    prefill(IC<0>{}, IC<2>{});
    wait_vm<4>();
    if (lane == 0) __hip_atomic_store((GAS unsigned long long*)(ws + WS_FLAG + FL_A) + b * 4 + w, tag, __ATOMIC_RELAXED, __HIP_MEMORY_SCOPE_AGENT);
  }
  STAMP(51);
  const int us0 = w * 256 + b, us = us0 < 1764 ? us0 : 0, uq0 = (7 - w) * 256 + b, uq = uq0 < 576 ? uq0 : 0;
  const int ty = us < 1296 ? 0 : us < 1476 ? 1 : 2;
  const int su = ty == 0 ? us : ty == 1 ? us - 1296 : us - 1476;
  const int kap = ty < 2 ? su / 9 : (su % 144) >> 3, lc = ty < 2 ? su % 9 : 64 + (su & 7), mat = su / 144;
  const int hd = uq / 18, k5 = uq % 18;
  const char *sjsrc, *sqsrc; unsigned sjstep, voff_sj, voff_sq, qrd0, qrd1, sqlds = sb + A_Q + (w >= 5 ? w - 5 : 0) * 5120;
  { const unsigned stride = ty < 2 ? 1152 : 9216;
    const bool nat = ty == 2 || (ty == 1 && kap < 16);
    sjsrc = (const char*)(ty == 0 ? wd : ty == 1 ? wo : mat ? wu : wg) + lc * 128 +
            (size_t)(ty != 1 ? 32 * kap : kap < 16 ? 36 * kap : 144 * (kap - 16) + 16) * stride;
    sjstep = (nat ? 8 : ty == 0 ? 4 : 36) * stride;
    const int e = lane >> 3, pc = lane & 7;
    voff_sj = (nat ? e : 16 * (e >> 2) + (e & 3)) * stride + (((pc >> 1) ^ ((e >> 1) & 1)) * 32) + (pc & 1) * 16;
    sqsrc = (const char*)wqkv + (size_t)k5 * 32 * 3456 + ((36 * hd) & ~15);
    const int n = min(lane, 62), row = (n * 57) >> 9, ch = n - 9 * row;
    voff_sq = row * 3456 + ((ch * 11) >> 5) * (1152 - 48) + ch * 16;
    auto qrow = [&](int rho) { const int blk = rho < 28 ? (rho * 37) >> 8 : 4; return sqlds + blk * 1024 + (rho < 28 ? rho - 7 * blk : rho - 25) * 144 + ((36 * hd) & 8); };
    qrd0 = qrow(8 * g + q); qrd1 = qrow(8 * g + q + 4);
    spin(sjsrc); spin(sjstep); spin(sqsrc); spin(sqlds); opaque(voff_sj); opaque(voff_sq); opaque(qrd0); opaque(qrd1);
  }
  unsigned sjd[4], sqd;
  { const int k2 = kap < 128 ? kap >> 5 : (kap - 128) >> 2, step = kap < 128 ? 16 * ((kap >> 1) & 1) + 2 * ((kap >> 2) & 7) + (kap & 1) : 32 + ((kap - 128) & 3);
    UNROLL for (int t = 0; t < 4; ++t) { const int NT = 4 * lc + t;
      sjd[t] = ty == 0 ? (unsigned)WS_WD + (((NT / 9) * 4 + k2) * 36 + step) * 9216 + (NT % 9) * 1024
             : ty == 1 ? (unsigned)WS_WO + (NT * 20 + kap) * 1024 : (unsigned)WS_W2 + (((NT - 256) * 18 + kap) * 6 + 4 + mat) * 1024;
      spin(sjd[t]); }
    sqd = (unsigned)WS_W2 + (hd * 18 + k5) * 6144; spin(sqd); }
  unsigned son = (unsigned)((us0 - 1764) >> 31), qon = (unsigned)((uq0 - 576) >> 31); spin(son); spin(qon);
  int zk = ty == 1 ? (kap < 16 ? 1 : 2) : 0; spin(zk);
  SCHED();
  if (w != 3) prefill(IC<2>{}, IC<NPRE>{});
  STAMP(52);
  auto tags_ok = [&](const u32x4 v) -> bool { return __ballot(((v[0] ^ tlo) | (v[1] ^ thi) | (v[2] ^ tlo) | (v[3] ^ thi)) != 0u) == 0ull; };
  if (w == 3) {
    const int pw = max(32 * r - 2 + (int)min(lane, 33), 0);
    const char* fa = ws + WS_FLAG + FL_A;
    for (unsigned spins = 0; spins < 1000000u; ++spins) {
      u32x4 v0; u32x2 v1;
      asm volatile("global_load_dwordx4 %0, %2, %3 sc1\n\tglobal_load_dwordx2 %1, %2, %3 offset:16 sc1\n\ts_waitcnt vmcnt(0)" : "=&v"(v0), "=&v"(v1) : "v"(pw * 32), "s"(fa) : "memory");
      if (tags_ok(v0) && tags_ok((u32x4){v1[0], v1[1], v1[0], v1[1]})) break;
      __builtin_amdgcn_s_sleep(1);
    }
  }
  wait_vm<wait_at(-2)>();
  barrier();
  STAMP(53);

  bf16x8 xf[18][2];
  bf16x8 hf[4][4];
  bf16x8 wf[20];
  u32x2 xr[3];
  u32x4 pollv, pollw;
  const unsigned fo = (lane & 15) * 1152 + g * 8;
  auto ld_op = [&](auto kk) {
    constexpr Op o = P.op[decltype(kk)::v];
    if constexpr (o.kind == K_W) {
      constexpr int n = o.a, h = o.b;
      if constexpr (n < 18) big_dma(IC<n>{}, IC<h>{});
      else glds16<h * 1024, true>(g2src + (n - 18) * g2str, l16, g2lds + soff(n));
    } else if constexpr (o.kind == K_X) {
      constexpr int k2 = 2 * o.a + (o.b & 1), m = o.b >> 1;
      gload16<(o.b & 1) * 1024, true>(xf[k2][m], xbase + (m * 18 + 2 * o.a) * 1024, l16);
    } else if constexpr (o.kind == K_SJ) {
      glds16(sjsrc + o.a * sjstep, (o.a & 1) ? voff_sj ^ 64u : voff_sj, ldw * 4 - 3 * sb + (A_S + o.a * 1024));
    } else if constexpr (o.kind == K_FILL) {
      gstore16_sc1<o.a * 1024>(inbox + w * 4096, l16, (u32x4){~0u, ~0u, ~0u, ~0u});
    } else if constexpr (o.kind == K_CW) {
      constexpr int n = o.a, j = o.b; constexpr unsigned so = (n % 4) * CSTG;
      if constexpr (j < 4) glds16<j * 1024, true>(wdb + ((size_t)n * CSTG + w * 4096), l16, sb + so + w * 4096);
      else glds16<0, true, true>(wdb + ((size_t)n * CSTG + 32768 + w * 512), l16, sb + so + 32768 + w * 512);
    } else if constexpr (o.kind == K_CH) {
      gload16<o.b * 1024, true>(hf[o.a % 4][o.b], hb + (size_t)o.a * 8192, l16);
    } else if constexpr (o.kind == K_CO) {
      if constexpr (o.a < 4) glds16<o.a * 1024, true>(olb + w * 4096, l16, sb + C_O + w * 4096);
      else glds16<0, true>(olb + 32768 + w * 1024, l16, sb + C_O + 32768 + w * 1024);
    } else if constexpr (o.kind == K_C8) {
      if constexpr (o.a < 2) glds16<o.a * 1024, true>(wo8b + w * 2048, l16, sb + C_W8 + w * 2048);
      else glds16<0, true, true>(wo8b + 16384 + w * 512, l16, sb + C_W8 + 16384 + w * 512);
    } else if constexpr (o.kind == K_WF) {
      gload16<(o.a & 3) * 1024, true>(wf[o.a], wob + (o.a & ~3) * 1024, l16);
    } else if constexpr (o.kind == K_XL) {
      gload8(xr[o.a], xrb + (o.a == 0 ? w * 32 : o.a == 1 ? w * 32 + 16 * 1152 : 256 + kh * 16 * 1152), fo);
    }
  };
  auto ld_all = [&](auto ii) { constexpr int I = decltype(ii)::v; sfor<first_op(I), first_op(I + 1)>([&](auto kk) { ld_op(kk); }); };
  auto ld_spread = [&](auto ii, auto ff, auto nf) {
    constexpr int I = decltype(ii)::v, fi = decltype(ff)::v, NF = decltype(nf)::v, NL = nld_at(I), F0 = first_op(I);
    sfor<0, NL>([&](auto kk) { if constexpr (decltype(kk)::v * (NF - 1) / (NL > 0 ? NL : 1) == fi) { ld_op(IC<F0 + decltype(kk)::v>{}); SCHED(); } });
  };
  auto poll_rq = [&](auto vv) {
    constexpr int V = decltype(vv)::v;
    const unsigned off = V == 1 ? l16 : (unsigned)(((lane & 16) ? ((xcd >> 2) * 4 + ct) : (rt >> 1)) * 256 + (lane & 15) * 16);
    const char* fp = flg + (V - 1) * 2048;
    gload16_rw<0>(pollv, fp, off);
    if constexpr (V == 1) gload16_rw<1024>(pollw, fp, off);
  };
  auto end_item = [&](auto ii) {
    constexpr int I = decltype(ii)::v, J = I + 1;
    if constexpr (wait_at(I) < 63) wait_vm<wait_at(I)>();
    if constexpr (J >= 0 && J < 9) { pin(xf[2 * J][0]); pin(xf[2 * J][1]); pin(xf[2 * J + 1][0]); pin(xf[2 * J + 1][1]); }
    if constexpr (J >= IT_C && J < IT_C + 9) { UNROLL for (int j = 0; j < 4; ++j) pin(hf[(J - IT_C) % 4][j]); }
    sfor<1, 4>([&](auto vv) { constexpr int V = decltype(vv)::v; if constexpr (I == CHECK_AT[V]) {
      if (w == 0) {
        wait_vm<after_body(POLL_AT[V] - 1, I) - body_at(POLL_AT[V])>();
        pin(pollv); if constexpr (V == 1) pin(pollw);
        unsigned spins = 0;
        while (!(tags_ok(pollv) && (V != 1 || tags_ok(pollw)))) {
          __builtin_amdgcn_s_sleep(4);
          poll_rq(vv);
          wait0_pin(pollv); if constexpr (V == 1) pin(pollw);
          if (++spins > 1000000u) break;
        }
        if constexpr (V == 1) {
          if (b == 0 && lane == 0) __hip_atomic_store((GAS unsigned long long*)(flg + FL_EPOCH), tag, __ATOMIC_RELAXED, __HIP_MEMORY_SCOPE_AGENT); }
      } } });
  };
  unsigned ctv = 0;
  auto code_touch = [&](int ahead) {
    if (w == 7) { unsigned long long pc; asm volatile("s_getpc_b64 %0" : "=s"(pc)); gtouch(ctv, (const char*)pc + ahead, lane * 128u); }
    SCHED(); };
  auto start_item = [&](auto ii) {
    constexpr int I = decltype(ii)::v;
    if constexpr (BAR[I] && I > 0) barrier();
    if constexpr (I == 3 || I == 12 || I == 22 || I == 30) code_touch(I == 30 ? 3072 : 4096);
    sfor<1, 4>([&](auto vv) { constexpr int V = decltype(vv)::v;
      if constexpr (I == FLAG_AT[V]) {
        if (tid == 64) __hip_atomic_store((GAS unsigned long long*)flg + (V - 1) * 256 + r * 32 + c, tag, __ATOMIC_RELAXED, __HIP_MEMORY_SCOPE_AGENT);
        SCHED(); }
      if constexpr (I == POLL_AT[V]) { pollv = (u32x4){0u, 0u, 0u, 0u}; pin(pollv); if constexpr (V == 1) { pollw = pollv; pin(pollw); } if (w == 0) poll_rq(vv); SCHED(); } });
    STAMP(I + 1);
  };

  unsigned tb[5];
  auto set_big = [&](unsigned sb) { const int f = (q >> 1) | ((g & 1) << 1);
    UNROLL for (int t = 0; t < 4; ++t) { tb[t] = sb + (8 * g + q) * 128 + ((t ^ f) * 32) + 8 * p; opaque(tb[t]); } };
  auto set_g2 = [&](unsigned sb) { UNROLL for (int t = 0; t < 4; ++t) { tb[t] = sb + l16 + (t ^ (w & 3)) * 1024; opaque(tb[t]); } tb[4] = sb + l16; opaque(tb[4]); };
  auto rebase = [&](auto aa, auto bb) { constexpr int n0 = decltype(aa)::v, n1 = decltype(bb)::v;
    if constexpr (region(n0) != region(n1)) { UNROLL for (int t = 0; t < (n1 < 18 ? 4 : 5); ++t) { tb[t] += region(n1) - region(n0); opaque(tb[t]); } } };
  auto baddr = [&](auto nn, auto ff, auto hh) -> unsigned {
    constexpr int n = decltype(nn)::v, fi = decltype(ff)::v, i = decltype(hh)::v; constexpr unsigned so = soff(n) - region(n);
    return tb[fi & 3] + (so + ((fi >> 2) & 1) * 8192 + (fi >> 3) * 4096 + i * 512); };
  auto g2rd = [&](auto nn, auto ff) -> bf16x8 {
    constexpr int n = decltype(nn)::v, fi = decltype(ff)::v; constexpr unsigned so = soff(n) - region(n);
    if constexpr (fi < 12 && fi % 6 < 4) return *(LAS bf16x8*)(size_t)(tb[fi % 6] + (so + (fi / 6) * 6144));
    else return *(LAS bf16x8*)(size_t)(tb[4] + (so + fi * 1024)); };

  f32x4 acc[8][2], acch, acy[9][2];
  bf16x8 af[4];
  bf16x8 hfr;
  auto first3 = [&](auto nn) {
    constexpr int n = decltype(nn)::v;
    SCHED();
    if constexpr (n >= 18) hfr = g2rd(nn, IC<12>{});
    sfor<0, 3>([&](auto ff) { if constexpr (n < 18) af[decltype(ff)::v] = cat(trd(baddr(nn, ff, IC<0>{})), trd(baddr(nn, ff, IC<1>{}))); else af[decltype(ff)::v] = g2rd(nn, ff); });
    SCHED();
  };
  u32x4 sjf;
  auto side_read = [&](auto tt, auto nn) {
    constexpr int t = decltype(tt)::v;
    if constexpr (t < 4) { const unsigned a = tb[t] + (ldw - sb) * 4 + (A_S - region(decltype(nn)::v)); sjf = __builtin_bit_cast(u32x4, cat(trd(a), trd(a + 512))); }
    else { const int G = min(4 * (t - 4) + p, 14); const unsigned a = ((G * 52) >> 8) * 8 + 8 * G; sjf = __builtin_bit_cast(u32x4, cat(trd(qrd0 + a), trd(qrd1 + a))); }
    SCHED();
  };
  auto side_store = [&](auto tt) {
    constexpr int t = decltype(tt)::v;
    if constexpr (t < 4) { sjf[0] = (zk == 1 && g == 2) ? 0u : sjf[0]; sjf[1] = zk == 2 ? 0u : sjf[1]; }
    char* dst = ws + (t < 4 ? sjd[t & 3] : sqd + (t & 3) * 1024);
    const unsigned on = t < 4 ? son : qon; const u32x4 v = sjf;
    asm volatile("s_mov_b32 exec_lo, %3\n\ts_mov_b32 exec_hi, %3\n\tglobal_store_dwordx4 %0, %1, %2 sc1\n\ts_mov_b64 exec, -1\n\ts_nop 0" ::"v"(l16), "v"(v), "s"(dst), "s"(on) : "memory");
    SCHED();
  };
  auto run_stage = [&](auto nn, auto ii) {
    constexpr int n = decltype(nn)::v, NF = nfrag(n);
    constexpr bool big = n < 18;
    constexpr int st = n % 9;
    constexpr bool last = st == 8;
    constexpr bool has_next = !last && !late(n + 1);
    sfor<0, NF>([&](auto ff) {
      constexpr int fi = decltype(ff)::v;
      constexpr int kstep = 2 * st + (big ? fi >> 3 : fi / 6), ai = big ? (fi & 7) : fi % 6;
      constexpr bool pf = (fi + 3 < NF) || has_next;
      constexpr int pn = fi + 3 < NF ? n : n + 1, pfi = fi + 3 < NF ? fi + 3 : fi + 3 - NF;
      const bf16x8 cur = af[fi & 3];
      mma<kstep == 0>(acc[ai][0], cur, xf[kstep][0]);
      SCHED();
      if constexpr (pf && pn != n && pfi == 0) rebase(nn, IC<pn>{});
      if constexpr (big) {
        s16x4 lo, hi;
        if constexpr (pf) lo = trd(baddr(IC<pn>{}, IC<pfi>{}, IC<0>{}));
        SCHED();
        mma<kstep == 0>(acc[ai][1], cur, xf[kstep][1]);
        SCHED();
        if constexpr (pf) { hi = trd(baddr(IC<pn>{}, IC<pfi>{}, IC<1>{})); af[(fi + 3) & 3] = cat(lo, hi); }
        SCHED();
      } else {
        if constexpr (pf) af[(fi + 3) & 3] = g2rd(IC<pn>{}, IC<pfi>{});
        SCHED();
        mma<kstep == 0>(acc[ai][1], cur, xf[kstep][1]);
        SCHED();
        if constexpr (ai == 0) { mma<kstep == 0>(acch, cur, hfr); SCHED(); }
        if constexpr (fi == 1) { hfr = g2rd(nn, IC<13>{}); SCHED(); }
        if constexpr (fi == 10 && has_next) { hfr = g2rd(IC<n + 1>{}, IC<12>{}); SCHED(); }
      }
      ld_spread(ii, ff, IC<NF>{});
      if constexpr (n < 5 && fi == 14) {
        const char* src = sqsrc + (n < 4 ? 7 * n : 25) * 3456; const unsigned dst = sqlds + n * 1024;
        asm volatile("s_mov_b32 m0, %2\n\ts_mov_b32 exec_lo, %3\n\ts_mov_b32 exec_hi, %3\n\tglobal_load_lds_dwordx4 %0, %1\n\ts_mov_b64 exec, -1" ::"v"(voff_sq), "s"(src), "s"(dst), "s"(qon) : "memory");
        SCHED(); }
      if constexpr (n >= SJ0 && n < SJ0 + 3 && fi < 15) {
        constexpr int u = (n - SJ0) * 3 + fi / 5;
        if constexpr (u < 8 && fi % 5 == 0) side_read(IC<(u + 4) & 7>{}, nn);
        if constexpr (u < 8 && fi % 5 == 4) side_store(IC<(u + 4) & 7>{});
      }
    });
    if constexpr (last) mma_drain();
    if constexpr (!last && !has_next) rebase(nn, IC<n + 1>{});
  };
  auto ep_unit = [&](auto gg, auto uu) {
    constexpr int gi = decltype(gg)::v, T = decltype(uu)::v >> 1, m = decltype(uu)::v & 1;
    gstore16_sc1<(T * 2 + m) * 1024>(hbase + gi * 32768, l16, silu_mul8(acc[2 * T][m], acc[4 + 2 * T][m], acc[2 * T + 1][m], acc[5 + 2 * T][m]));
  };

  ld_all(IC<-1>{});
  set_big(sb + region(0));
  first3(IC<0>{});
  end_item(IC<-1>{});
  sfor<0, IT_C>([&](auto ii) {
    constexpr int I = decltype(ii)::v; constexpr int n = stage_at(I);
    start_item(ii);
    if constexpr (n >= 0) {
      if constexpr (late(n) && n > 0) first3(IC<n>{});
      run_stage(IC<n>{}, ii);
    } else {
      ld_all(ii);
      if constexpr (I == 9) {
        sfor<0, 4>([&](auto uu) { ep_unit(IC<0>{}, uu); });
        rebase(IC<8>{}, IC<9>{}); first3(IC<9>{});
      } else if constexpr (I == 19) {
        sfor<0, 4>([&](auto uu) { ep_unit(IC<1>{}, uu); });
        set_g2(sb + region(18)); first3(IC<18>{});
      } else if constexpr (I == 29) {
        UNROLL for (int t = 0; t < 4; ++t) {
          const int G = 4 * (t ^ (w & 3)) + g;
          const unsigned a = sb + DUMP + (16 + 32 * w + (lane & 15)) * ROWB + (G < 15 ? ((G * 52) >> 8) * 8 + 8 * G : 88);
          UNROLL for (int m = 0; m < 2; ++m)
            *(LAS u32x2*)(size_t)(a + m * 16 * ROWB) = (u32x2){pk_bf16(acc[t][m][0], acc[t][m][1]), pk_bf16(acc[t][m][2], acc[t][m][3])};
          if (t == 0 && w < 4) *(LAS u32x2*)(size_t)(a - (16 + 32 * w) * ROWB) = (u32x2){pk_bf16(acch[0], acch[1]), pk_bf16(acch[2], acch[3])};
        }
        gstore16_sc1(hbase + (65536 - (c & 7) * 3072), l16, silu_mul8(acc[4][0], acc[5][0], acc[4][1], acc[5][1]));
        asm volatile("s_waitcnt lgkmcnt(0)" ::: "memory");
      }
    }
    end_item(ii);
  });

  bf16x8 kf[3]; u32x4 qf[2]; s16x4 vt[3][2]; f32x4 sa[2], sc[2]; float s0[2], s1[2], amx[2];
  auto att_seg = [&](auto ss) {
    constexpr int SEG = decltype(ss)::v;
    const int m = lane & 15;
    const bool hi = m & 1, b0 = 4 * g + (m & 1) <= m, b1 = 4 * g + 2 + (m & 1) <= m;
    if constexpr (SEG == 0) {
      const unsigned rowa = sb + DUMP + (32 * w + m) * ROWB + (g < 3 ? g : 0) * 16;
      const unsigned tra = sb + DUMP + (32 * w + 4 * g + q) * ROWB + 96 + 8 * p;
      UNROLL for (int t = 0; t < 3; ++t) kf[t] = *(LAS bf16x8*)(size_t)(rowa + 48 + t * 16 * ROWB);
      UNROLL for (int t = 0; t < 2; ++t) qf[t] = *(LAS u32x4*)(size_t)(rowa + (t + 1) * 16 * ROWB);
      UNROLL for (int t = 0; t < 3; ++t) UNROLL for (int d = 0; d < 2; ++d) vt[t][d] = trd(tra + t * 16 * ROWB + d * 32);
    } else if constexpr (SEG == 1) {
      const bool odd = c & 1;
      const unsigned m0 = (g == 3 || (odd && g == 0)) ? 0u : ~0u, m1 = (g == 3 || (!odd && g == 2)) ? 0u : ~0u, m2 = g >= 2 ? 0u : ~0u;
      UNROLL for (int t = 0; t < 2; ++t) {
        const u32x4 qm = {qf[t][0] & m0, qf[t][1] & m1, qf[t][2] & m2, qf[t][3] & m2};
        sa[t] = mfma(kf[t], __builtin_bit_cast(bf16x8, qm), zero4); sc[t] = mfma(kf[t + 1], __builtin_bit_cast(bf16x8, qm), zero4);
      }
    } else if constexpr (SEG == 3) {
      const float K = 0.23570226039551584f * 1.4426950408889634f;
      UNROLL for (int t = 0; t < 2; ++t) {
        float e0 = sa[t][0], e2 = sa[t][2], f0 = sc[t][0], f2 = sc[t][2]; opaque(e0); opaque(e2); opaque(f0); opaque(f2);
        const float a0 = hi ? sa[t][1] : e0, a1 = hi ? sa[t][3] : e2, c0 = hi ? sc[t][1] : f0, c1 = hi ? sc[t][3] : f2;
        s0[t] = (b0 ? c0 : a0) * K; s1[t] = (b1 ? c1 : a1) * K;
        if (t == 0 && r == 0 && w == 0) { s0[t] = b0 ? s0[t] : -1.4426950e9f; s1[t] = b1 ? s1[t] : -1.4426950e9f; }
        amx[t] = xor32_max(xor16_max(vmax(s0[t], s1[t])));
      }
    } else if constexpr (SEG == 4) {
      UNROLL for (int t = 0; t < 2; ++t) {
        s0[t] = __builtin_amdgcn_exp2f(s0[t] - amx[t]); s1[t] = __builtin_amdgcn_exp2f(s1[t] - amx[t]);
        amx[t] = __builtin_amdgcn_rcpf(xor32_add(xor16_add(s0[t] + s1[t])));
      }
    } else if constexpr (SEG == 5) {
      UNROLL for (int t = 0; t < 2; ++t) {
        const unsigned t0 = pk_bf16(s0[t] * amx[t], 0.f) << (hi ? 16 : 0), t1 = pk_bf16(s1[t] * amx[t], 0.f) << (hi ? 16 : 0);
        const u32x4 pf = {b0 ? 0u : t0, b1 ? 0u : t1, b0 ? t0 : 0u, b1 ? t1 : 0u};
        sa[t] = mfma(cat(vt[t][0], vt[t + 1][0]), __builtin_bit_cast(bf16x8, pf), zero4);
        sc[t] = mfma(cat(vt[t][1], vt[t + 1][1]), __builtin_bit_cast(bf16x8, pf), zero4);
      }
    } else if constexpr (SEG == 6) {
      const u32x4 o = pk8(sa[0], sa[1]);
      const auto r0 = __builtin_amdgcn_permlane16_swap(o[0], o[2], false, false), r1 = __builtin_amdgcn_permlane16_swap(o[1], o[3], false, false);
      gstore16_sc1(osb + c * 1024, l16, (u32x4){r0[0], r1[0], r0[1], r1[1]});
      if (g == 0) gstore16_sc1(osb + 32768 + c * 256, l16, pk8(sc[0], sc[1]));
    }
    SCHED();
  };

  unsigned ab[4];
  ab[0] = sb + kh * (18432 + 5120) + l16; ab[1] = sb + kh * (18432 - 4096) + l16; ab[2] = ab[0] + 2 * CSTG; ab[3] = ab[1] + 2 * CSTG;
  UNROLL for (int j = 0; j < 4; ++j) opaque(ab[j]);
  auto slot_of = [](int n, int fi) constexpr { return (n == 8 && fi >= 9) ? (fi - 9 + 4) % 9 : fi % 9; };
  auto ard = [&](auto nn, auto ff) -> bf16x8 { constexpr int n = decltype(nn)::v, fi = decltype(ff)::v, sl = slot_of(n, fi);
    return *(LAS bf16x8*)(size_t)(ab[((n % 4) >= 2 ? 2 : 0) + (sl >= 4)] + ((n % 2) * CSTG + (fi / 9) * 9216 + sl * 1024)); };
  unsigned xa = sb + C_X + l16 + w * 5120;
  sfor<0, 9>([&](auto nn) {
    constexpr int n = decltype(nn)::v, I = IT_C + n;
    start_item(IC<I>{});
    if constexpr (clate(n)) { sfor<0, 3>([&](auto ff) { af[(n * 18 + decltype(ff)::v) & 3] = ard(nn, ff); }); SCHED(); }
    if constexpr (n == 8) {
      UNROLL for (int j = 0; j < 4; j += 2) {
        const bf16x8 a = hf[0][j], bb = hf[0][j + 1];
        hf[0][j] = __builtin_shufflevector(a, bb, 0, 1, 2, 3, 8, 9, 10, 11); hf[0][j + 1] = __builtin_shufflevector(a, bb, 4, 5, 6, 7, 12, 13, 14, 15);
        pin(hf[0][j]); pin(hf[0][j + 1]); }
      opaque(xa);
      asm volatile("s_nop 1"); SCHED(); }
    sfor<0, 18>([&](auto ff) {
      constexpr int fi = decltype(ff)::v, gf = n * 18 + fi, sub = fi / 9, nt = slot_of(n, fi);
      constexpr bool pf = (fi + 3 < 18) || (n < 8 && !clate(n + 1));
      constexpr int pn = fi + 3 < 18 ? n : n + 1, pfi = fi + 3 < 18 ? fi + 3 : fi + 3 - 18;
      const bf16x8 cur = af[gf & 3];
      mma<(n == 0 && sub == 0)>(acy[nt][0], cur, hf[n % 4][2 * sub]);
      SCHED();
      if constexpr (pf) af[(gf + 3) & 3] = ard(IC<pn>{}, IC<pfi>{});
      SCHED();
      mma<(n == 0 && sub == 0)>(acy[nt][1], cur, hf[n % 4][2 * sub + 1]);
      SCHED();
      ld_spread(IC<I>{}, ff, IC<(n == 8 ? 12 : 18)>{});
      if constexpr (n == 8 && fi >= 13) {
        *(LAS u32x4*)(size_t)(xa + (fi - 13) * 1024) = pk8(acy[fi - 9][0], acy[fi - 9][1]); SCHED(); }
      if constexpr (gf == 0) att_seg(IC<0>{});
      if constexpr (gf == 4) att_seg(IC<1>{});
      if constexpr (gf == 8) att_seg(IC<3>{});
      if constexpr (gf == 13) att_seg(IC<4>{});
      if constexpr (gf == 19) att_seg(IC<5>{});
      if constexpr (gf == 24) att_seg(IC<6>{});
    });
    end_item(IC<I>{});
  });
  mma_drain();
  opaque(ctv);
  STAMP(40);
  if (wm == ks) { UNROLL for (int j = 0; j < 4; ++j) *(LAS u32x4*)(size_t)(sb + C_OWN + l16 + (kh * 4 + j) * 1024) = pk8(acy[j][0], acy[j][1]); }
  asm volatile("s_waitcnt lgkmcnt(0)" ::: "memory");
  barrier();
  STAMP(41);
  f32x4 sv0, sv1, sv2 = zero4;
  unsigned oa = sb + C_O + g * 512 + (lane & 15) * 16; opaque(oa);
  auto ord = [&](auto ss, unsigned ra, unsigned rb, int t) -> bf16x8 {
    constexpr int s = decltype(ss)::v;
    if constexpr (s < 16) return *(LAS bf16x8*)(size_t)(ra + s * 2048 + t * 256);
    else { const unsigned a = rb + 32768 + (s - 16) * 2048 + t * 8; return cat(*(LAS s16x4*)(size_t)a, *(LAS s16x4*)(size_t)(a + 256)); } };
  if (wm != ks) {
    const unsigned pa0 = sb + C_X + l16 + (w ^ 4) * 5120 + kh * 1024;
    u32x4 v[5];
    UNROLL for (int j = 0; j < 5; ++j) v[j] = *(LAS u32x4*)(size_t)(pa0 + j * 1024);
    sfor<0, 4>([&](auto jj) { constexpr int j = decltype(jj)::v; gstore16_sc1<j * 1024>(pubb, l16, pk8(acy[j][0] + up_lo(v[j]), acy[j][1] + up_hi(v[j]))); });
    if (kh == 0) gstore16_sc1(pubb + 4096, l16, pk8(acy[4][0] + up_lo(v[4]), acy[4][1] + up_hi(v[4])));
    wait_vm<4>();
  } else {
    bf16x8 w8[4], o8[4];
    const unsigned wa = sb + l16 + C_W8, ra = oa + kh * 256, rb = oa + kh * 8;
    sfor<0, 3>([&](auto ss) { constexpr int s = decltype(ss)::v; w8[s] = *(LAS bf16x8*)(size_t)(wa + s * 1024); o8[s] = ord(ss, ra, rb, 0); });
    SCHED();
    sfor<0, 20>([&](auto ss) { constexpr int s = decltype(ss)::v;
      if constexpr (s + 3 < 20) { w8[(s + 3) & 3] = *(LAS bf16x8*)(size_t)(wa + (s + 3) * 1024); o8[(s + 3) & 3] = ord(IC<s + 3>{}, ra, rb, 0); }
      SCHED();
      sv2 = mfma(w8[s & 3], o8[s & 3], sv2); SCHED(); });
    wait_vm<0>();
  }
  UNROLL for (int s = 0; s < 20; ++s) pin(wf[s]);
  UNROLL for (int j = 0; j < 3; ++j) pin(xr[j]);
  STAMP(42);
  { bf16x8 of[4][2];
    sfor<0, 3>([&](auto ss) { of[decltype(ss)::v][0] = ord(ss, oa, oa, 0); SCHED(); of[decltype(ss)::v][1] = ord(ss, oa, oa, 1); SCHED(); });
    sfor<0, 20>([&](auto ss) { constexpr int s = decltype(ss)::v;
      mma<s == 0>(sv0, wf[s], of[s & 3][0]); SCHED();
      if constexpr (s + 3 < 20) of[(s + 3) & 3][0] = ord(IC<s + 3>{}, oa, oa, 0);
      SCHED();
      mma<s == 0>(sv1, wf[s], of[s & 3][1]); SCHED();
      if constexpr (s + 3 < 20) of[(s + 3) & 3][1] = ord(IC<s + 3>{}, oa, oa, 1);
      SCHED(); }); }
  auto own = [&](int t, int h) -> u32x4 { const int sl = h ? (t + 4) % 9 : t;
    return *(LAS u32x4*)(size_t)(sb + l16 + (sl < 4 ? C_OWN + (h * 4 + sl) * 1024 : C_X + (ks + 4 * h) * 5120 + (sl - 4) * 1024)); };
  const u32x4 oa0 = own(w, 0), oa1 = own(w, 1), ob0 = own(8, 0), ob1 = own(8, 1);
  mma_drain();
  STAMP(43);
  u32x4 pa[3], pb[3];
  auto fetch = [&]() {
    const char* s0 = inbox + w * 1024; const char* s1 = s0 + 9216; const char* s2 = s0 + 18432;
    const char* t0 = inbox + 8192; const char* t1 = t0 + 9216; const char* t2 = t0 + 18432;
    int dk = wm ^ ks; spin(dk); const unsigned on = (unsigned)((dk - 1) >> 31);
    asm volatile("global_load_dwordx4 %0, %6, %7 sc1\n\tglobal_load_dwordx4 %1, %6, %8 sc1\n\tglobal_load_dwordx4 %2, %6, %9 sc1\n\t"
                 "s_mov_b32 exec_lo, %13\n\ts_mov_b32 exec_hi, %13\n\t"
                 "global_load_dwordx4 %3, %6, %10 sc1\n\tglobal_load_dwordx4 %4, %6, %11 sc1\n\tglobal_load_dwordx4 %5, %6, %12 sc1\n\t"
                 "s_mov_b64 exec, -1\n\ts_waitcnt vmcnt(0)"
                 : "=&v"(pa[0]), "=&v"(pa[1]), "=&v"(pa[2]), "+v"(pb[0]), "+v"(pb[1]), "+v"(pb[2])
                 : "v"(l16), "s"(s0), "s"(s1), "s"(s2), "s"(t0), "s"(t1), "s"(t2), "s"(on) : "memory"); };
  UNROLL for (int j = 0; j < 3; ++j) pb[j] = (u32x4){0u, 0u, 0u, 0u};
  for (unsigned spins = 0; spins < 200000u; ++spins) {
    fetch();
    unsigned mx = 0;
    UNROLL for (int j = 0; j < 3; ++j) {
      mx = max(max(mx, max(pa[j][0], pa[j][1])), max(pa[j][2], pa[j][3])); mx = max(max(mx, max(pb[j][0], pb[j][1])), max(pb[j][2], pb[j][3])); }
    if (__ballot(mx == ~0u) == 0ull) break;
    __builtin_amdgcn_s_sleep(2);
  }
  STAMP(44); STAMP(45);
  f32x4 y0 = up_lo(oa0) + up_lo(oa1), y1 = up_hi(oa0) + up_hi(oa1);
  UNROLL for (int j = 0; j < 3; ++j) { y0 += up_lo(pa[j]); y1 += up_hi(pa[j]); }
  auto fin = [&](f32x4 y, f32x4 s, u32x2 xv) -> u32x2 {
    u32x2 o = {pk_bf16(bf_lo(xv[0]) + silu_mul(y[0], s[0]), bf_hi(xv[0]) + silu_mul(y[1], s[1])),
               pk_bf16(bf_lo(xv[1]) + silu_mul(y[2], s[2]), bf_hi(xv[1]) + silu_mul(y[3], s[3]))};
    return o; };
  *(u32x2*)(outb + w * 32 + fo) = fin(y0, sv0, xr[0]);
  *(u32x2*)(outb + w * 32 + 16 * 1152 + fo) = fin(y1, sv1, xr[1]);
  if (wm == ks) {
    f32x4 y2 = kh ? up_hi(ob0) + up_hi(ob1) : up_lo(ob0) + up_lo(ob1);
    UNROLL for (int j = 0; j < 3; ++j) y2 += kh ? up_hi(pb[j]) : up_lo(pb[j]);
    *(u32x2*)(outb + 256 + kh * 16 * 1152 + fo) = fin(y2, sv2, xr[2]);
  }
  STAMP(46);
  STAMP(47); STAMP_RT(63); STAMP_OUT(0);
}

extern "C" void kernel_launch(void* const* d_in, const int* in_sizes, int n_in, void* d_out, int out_size,
                              void* d_ws, size_t ws_size, hipStream_t stream) {
  const u16* x = (const u16*)d_in[0]; const u16* wqkv = (const u16*)d_in[1]; const u16* wo = (const u16*)d_in[2];
  const u16* wg = (const u16*)d_in[4]; const u16* wu = (const u16*)d_in[5]; const u16* wd = (const u16*)d_in[6];
  (void)hipFuncSetAttribute((const void*)main_kernel, hipFuncAttributeMaxDynamicSharedMemorySize, pg::LDS_BYTES);
  main_kernel<<<256, 512, pg::LDS_BYTES, stream>>>(x, wqkv, wo, wg, wu, wd, (u16*)d_out, (char*)d_ws);
}
```
